```python
import torch

def _softmax(x, axis=-1):
    return torch.softmax(x, dim=axis)
def _gelu(x):
    return torch.nn.functional.gelu(x, approximate="tanh")
def _silu(x):
    return torch.nn.functional.silu(x)
def _sigmoid(x):
    return torch.sigmoid(x)
def _layernorm(x, axis=-1, eps=1e-5):
    m = x.mean(dim=axis, keepdim=True)
    v = x.var(dim=axis, keepdim=True, unbiased=False)
    return (x - m) / torch.sqrt(v + eps)
def _scatter_add(tgt, idx, src, axis=0):
    return tgt.clone().index_add_(axis, idx, src)
def _segment_sum(src, seg, n):
    out = torch.zeros((n,) + src.shape[1:], dtype=src.dtype)
    return out.index_add_(0, seg, src)
def _pad(x, pw):
    flat = tuple(v for pair in reversed(pw) for v in pair)
    return torch.nn.functional.pad(x, flat)

kernel_name = 'standalone__gqa_block_cached__near_ridge'

def setup_inputs():

    _w = (lambda a: (lambda t: t.to(torch.bfloat16) if t.is_floating_point() else t)(torch.as_tensor(a)))
    B, Sq, D = 8, 48, 1024
    Hq, Hkv, Sk, Dh = 32, 4, 2048, 128
    assert Hq % Hkv == 0, (Hq, Hkv)
    return {
        "x": _w((lambda *s: torch.randn(*s, dtype=torch.bfloat16) * 0.8)(B, Sq, D)),
        "k_cache": _w((lambda *s: torch.randn(*s, dtype=torch.bfloat16) * 0.8)(B, Hkv, Sk, Dh)),
        "v_cache": _w((lambda *s: torch.randn(*s, dtype=torch.bfloat16) * 0.05)(B, Hkv, Sk, Dh)),
        "wq": _w((lambda *s: torch.randn(*s, dtype=torch.bfloat16) * 0.8)(D, Hq * Dh) * (D ** -0.5)),
        "wo": _w((lambda *s: torch.randn(*s, dtype=torch.bfloat16) * 0.05)(Hq * Dh, D) * ((Hq * Dh) ** -0.5)),
    }

def reference(x, k_cache, v_cache, wq, wo):
    B, Sq, D = x.shape
    Hq, Dh = 32, 128
    Hkv = k_cache.shape[1]
    rep = Hq // Hkv
    q = (lambda x, *d: x.permute(*d))((x @ wq).reshape(B, Sq, Hq, Dh), 0, 2, 1, 3)
    k = (lambda x, n, axis: torch.repeat_interleave(x, n, dim=axis))(k_cache, rep, axis=1)
    v = (lambda x, n, axis: torch.repeat_interleave(x, n, dim=axis))(v_cache, rep, axis=1)
    scores = torch.einsum("bhqd,bhkd->bhqk", q, k) * (Dh ** -0.5)
    p = _softmax(scores, axis=-1)
    o = torch.einsum("bhqk,bhkd->bhqd", p, v)
    return (lambda x, *d: x.permute(*d))(o, 0, 2, 1, 3).reshape(B, Sq, Hq * Dh) @ wo
```

```cpp
#include <hip/hip_runtime.h>
#include <hip/hip_bf16.h>
#include <hip/hip_runtime.h>
#include <hip/hip_bf16.h>
#include <stdint.h>
namespace naive {
constexpr int B=8, SQ=48, D=1024, HQ=32, HKV=4, SK=2048, DH=128;
__device__ __forceinline__ float bf2f(uint16_t v){ return __uint_as_float(((uint32_t)v)<<16); }
__device__ __forceinline__ uint16_t f2bf(float f){ uint32_t u=__float_as_uint(f); u += 0x7fffu + ((u>>16)&1u); return (uint16_t)(u>>16); }
__device__ __forceinline__ float rbf(float f){ return bf2f(f2bf(f)); }

__global__ void gemm_naive(const uint16_t* A, const uint16_t* Bm, uint16_t* C, int M, int N, int K){
  int n = blockIdx.x*blockDim.x + threadIdx.x; int m = blockIdx.y;
  if(n>=N) return;
  float acc=0.f;
  for(int k=0;k<K;++k) acc += bf2f(A[(size_t)m*K+k])*bf2f(Bm[(size_t)k*N+n]);
  C[(size_t)m*N+n]=f2bf(acc);
}
template<bool ROUND>
__global__ void attn_naive(const uint16_t* Q, const uint16_t* Kc, const uint16_t* Vc, uint16_t* O){
  __shared__ float s[SK]; __shared__ float red[256]; __shared__ float q[DH];
  int idx=blockIdx.x; int qi=idx%SQ; int h=(idx/SQ)%HQ; int b=idx/(SQ*HQ); int kvh=h/(HQ/HKV);
  int t=threadIdx.x;
  if(t<DH) q[t]=bf2f(Q[(size_t)(b*SQ+qi)*(HQ*DH)+h*DH+t]);
  __syncthreads();
  const uint16_t* Kp=Kc+(size_t)(b*HKV+kvh)*SK*DH; const uint16_t* Vp=Vc+(size_t)(b*HKV+kvh)*SK*DH;
  float mx=-INFINITY;
  for(int k=t;k<SK;k+=256){ float a=0; for(int d=0;d<DH;++d) a+=q[d]*bf2f(Kp[(size_t)k*DH+d]);
    if(ROUND){ a=rbf(a); a=rbf(a*0.08838834764831845f);} else a*=0.08838834764831845f; s[k]=a; mx=fmaxf(mx,a);}
  red[t]=mx; __syncthreads();
  for(int o=128;o>0;o>>=1){ if(t<o) red[t]=fmaxf(red[t],red[t+o]); __syncthreads(); }
  mx=red[0]; __syncthreads();
  float sum=0; for(int k=t;k<SK;k+=256){ float e=expf(s[k]-mx); s[k]=e; sum+=e; }
  red[t]=sum; __syncthreads();
  for(int o=128;o>0;o>>=1){ if(t<o) red[t]+=red[t+o]; __syncthreads(); }
  sum=red[0]; __syncthreads();
  for(int k=t;k<SK;k+=256){ float p=s[k]/sum; s[k]=ROUND?rbf(p):p; }
  __syncthreads();
  if(t<DH){ float a=0; for(int k=0;k<SK;++k) a+=s[k]*bf2f(Vp[(size_t)k*DH+t]); O[(size_t)(b*SQ+qi)*(HQ*DH)+h*DH+t]=f2bf(a);}
}
template<bool ROUND>
static void run(const void* x,const void* k,const void* v,const void* wq,const void* wo,void* out,void* ws,hipStream_t st){
  uint16_t* Q=(uint16_t*)ws; uint16_t* O=Q+(size_t)B*SQ*HQ*DH;
  gemm_naive<<<dim3(HQ*DH/256,B*SQ),256,0,st>>>((const uint16_t*)x,(const uint16_t*)wq,Q,B*SQ,HQ*DH,D);
  attn_naive<ROUND><<<B*HQ*SQ,256,0,st>>>(Q,(const uint16_t*)k,(const uint16_t*)v,O);
  gemm_naive<<<dim3(D/256,B*SQ),256,0,st>>>(O,(const uint16_t*)wo,(uint16_t*)out,B*SQ,D,HQ*DH);
}
}
extern "C" void kernel_launch(void* const* d_in, const int* in_sizes, int n_in,
                              void* d_out, int out_size, void* d_ws, size_t ws_size,
                              hipStream_t stream) {
  naive::run<false>(d_in[0], d_in[1], d_in[2], d_in[3], d_in[4], d_out, d_ws, stream);
}
```

```cpp
#include <hip/hip_runtime.h>
#include <hip/hip_bf16.h>
#include <stdint.h>

namespace k1 {
constexpr int B = 8, SQ = 48, DM = 1024, HQ = 32, HKV = 4, SK = 2048, DH = 128;
typedef __bf16 bf16x8 __attribute__((ext_vector_type(8)));
typedef __bf16 bf16x4 __attribute__((ext_vector_type(4)));
typedef __bf16 bf16x2 __attribute__((ext_vector_type(2)));
typedef short s16x4 __attribute__((ext_vector_type(4)));
typedef float f32x4 __attribute__((ext_vector_type(4)));
typedef float f32x2 __attribute__((ext_vector_type(2)));
typedef unsigned u32x4 __attribute__((ext_vector_type(4)));
typedef unsigned u32x2 __attribute__((ext_vector_type(2)));
#define LAS __attribute__((address_space(3)))
#define DI __device__ __forceinline__
typedef __amdgpu_buffer_rsrc_t srd_t;

DI unsigned rfl(unsigned x) { return __builtin_amdgcn_readfirstlane(x); }
DI srd_t mk_srd(const void* p, unsigned bytes) { return __builtin_amdgcn_make_buffer_rsrc((void*)p, (short)0, (int)bytes, 0x00020000); }
template <int NT>
DI void dma16(srd_t srd, unsigned voff, unsigned soff, unsigned ldsaddr) {
  if constexpr (NT) asm volatile("s_mov_b32 m0, %0\n\ts_nop 0\n\tbuffer_load_dwordx4 %1, %2, %3 offen lds nt" ::"s"(ldsaddr), "v"(voff), "s"(srd), "s"(soff) : "memory");
  else asm volatile("s_mov_b32 m0, %0\n\ts_nop 0\n\tbuffer_load_dwordx4 %1, %2, %3 offen lds" ::"s"(ldsaddr), "v"(voff), "s"(srd), "s"(soff) : "memory");
}
#define WAIT_VM(n) asm volatile("s_waitcnt vmcnt(" #n ")" ::: "memory")
#define WAIT_LGKM0() asm volatile("s_waitcnt lgkmcnt(0)" ::: "memory")
#define BARRIER() do { asm volatile("s_waitcnt lgkmcnt(0)" ::: "memory"); __builtin_amdgcn_s_barrier(); asm volatile("" ::: "memory"); } while (0)
DI f32x4 mfma16(bf16x8 a, bf16x8 b, f32x4 c) { return __builtin_amdgcn_mfma_f32_16x16x32_bf16(a, b, c, 0, 0, 0); }
DI bf16x8 lds_b128(unsigned addr) { return *(const LAS bf16x8*)(size_t)addr; }
DI s16x4 lds_tr(unsigned addr) { return __builtin_amdgcn_ds_read_tr16_b64_v4i16((LAS s16x4*)(size_t)addr); }
DI bf16x8 cat(s16x4 lo, s16x4 hi) { return __builtin_bit_cast(bf16x8, __builtin_shufflevector(lo, hi, 0, 1, 2, 3, 4, 5, 6, 7)); }
DI unsigned cvtpk(float lo, float hi) { f32x2 v = {lo, hi}; bf16x2 b = __builtin_convertvector(v, bf16x2); return __builtin_bit_cast(unsigned, b); }

#define ST(kern,i) do{}while(0)
constexpr unsigned LDS_X = 0;
constexpr unsigned LDS_W = 98304;
constexpr unsigned LDS_QS = 131072;
constexpr unsigned LDS_ST = 147456;
constexpr unsigned LDS_TOTAL = 163840;
constexpr float CSCALE = 0.12751743082f;
constexpr float THR = 8.f;

#define NT_KV 0
#define NT_WQ 0
#define NT_WO 0

template <int XMODE, int NTW>
DI void gemm_phase(f32x4 (&acc)[3], srd_t wsrd, unsigned wsoff0, unsigned wrow_stride, unsigned ldsb, int wid, int lane) {
  const int g = lane >> 4, qq = (lane & 15) >> 2, p = lane & 3;
  const unsigned wv = ((lane >> 2) & 7) * wrow_stride + (lane >> 5) * 64 + 16 * ((lane & 3) ^ ((2 * (wid & 1)) ^ ((lane >> 4) & 1)));
  auto issue_w = [&](int kt) {
    const unsigned so = wsoff0 + (unsigned)(kt * 64 + wid * 8) * wrow_stride;
    const unsigned dst = ldsb + LDS_W + (kt & 3) * 16384 + wid * 2048;
    dma16<NTW>(wsrd, wv, so, dst);
    dma16<NTW>(wsrd, wv, so + 128, dst + 1024);
  };
  issue_w(0); issue_w(1); issue_w(2);
  unsigned trb[2];
#pragma unroll
  for (int t = 0; t < 2; ++t) {
    const int ch = 2 * wid + (p >> 1);
    trb[t] = ldsb + LDS_W + 2048 * g + 512 * (ch >> 2) + 64 * (4 * t + qq) + 16 * ((ch & 3) ^ ((2 * g + t) & 3)) + 8 * (p & 1);
  }
  unsigned xb[2];
#pragma unroll
  for (int ks = 0; ks < 2; ++ks) {
    if constexpr (XMODE == 0) { const int row = lane & 15; xb[ks] = ldsb + LDS_X + row * 128 + 16 * ((4 * ks + g) ^ ((row >> 1) & 7)); }
    else xb[ks] = ldsb + LDS_X + ks * 3072 + lane * 16;
  }
#pragma unroll
  for (int qt = 0; qt < 3; ++qt) acc[qt] = f32x4{0.f, 0.f, 0.f, 0.f};
#pragma unroll
  for (int kt = 0; kt < 16; ++kt) {
    if (kt <= 13) WAIT_VM(4); else if (kt == 14) WAIT_VM(2); else WAIT_VM(0);
    BARRIER();
    if (kt + 3 < 16) issue_w(kt + 3);
#pragma unroll
    for (int ks = 0; ks < 2; ++ks) {
      s16x4 lo = lds_tr(trb[0] + (kt & 3) * 16384 + ks * 8192);
      s16x4 hi = lds_tr(trb[1] + (kt & 3) * 16384 + ks * 8192);
      bf16x8 a = cat(lo, hi);
#pragma unroll
      for (int qt = 0; qt < 3; ++qt) {
        bf16x8 b = lds_b128(xb[ks] + kt * 6144 + qt * (XMODE == 0 ? 2048 : 1024));
        acc[qt] = mfma16(a, b, acc[qt]);
      }
    }
  }
}

__global__ __launch_bounds__(512, 2) void attn_kernel(const char* __restrict__ x, const char* __restrict__ kc, const char* __restrict__ vc,
                                                      const char* __restrict__ wq, char* __restrict__ ofrag) {
  extern __shared__ __attribute__((aligned(16))) char smem[];
  const unsigned ldsb = (unsigned)(size_t)smem;
  const int tid = threadIdx.x, lane = tid & 63;
  const int wid = (int)rfl(tid >> 6);
  const int bid = blockIdx.x, xcd = bid & 7, slot = bid >> 3;
  const int kvh = xcd >> 1, b = (xcd & 1) * 4 + (slot >> 3), h = kvh * 8 + (slot & 7);
  const int g = lane >> 4, r16 = lane & 15;

  ST(0,0);
  {
    srd_t xs = mk_srd(x + (size_t)b * SQ * DM * 2, SQ * DM * 2);
#pragma unroll
    for (int u = 0; u < 12; ++u) {
      const int P = wid * 12 + u, kt = P / 6, j = P % 6;
      const unsigned voff = (lane >> 3) * 2048 + 16 * ((lane & 7) ^ ((4 * j + (lane >> 4)) & 7));
      dma16<0>(xs, voff, (unsigned)(j * 8 * 2048 + kt * 128), ldsb + LDS_X + P * 1024);
    }
  }
  f32x4 qacc[3];
  {
    srd_t ws = mk_srd(wq, DM * HQ * DH * 2);
    gemm_phase<0, NT_WQ>(qacc, ws, h * 256, HQ * DH * 2, ldsb, wid, lane);
  }
  BARRIER();
  ST(0,1);
  srd_t ks_ = mk_srd(kc + (size_t)(b * HKV + kvh) * SK * DH * 2, SK * DH * 2);
  srd_t vs_ = mk_srd(vc + (size_t)(b * HKV + kvh) * SK * DH * 2, SK * DH * 2);
  const unsigned kslot = ldsb + wid * 16384, vslot = kslot + 8192;
  const unsigned kv0 = (lane >> 4) * 256 + 16 * ((lane & 15) ^ (lane >> 4));
  const unsigned vv0 = ((lane >> 2) & 7) * 256 + (lane >> 5) * 64 + 16 * ((lane & 3) ^ ((lane >> 4) & 1));
  auto issue_k = [&](int blk) {
#pragma unroll
    for (int j = 0; j < 8; ++j) dma16<NT_KV>(ks_, kv0 ^ (64 * (j >> 1)), (unsigned)(blk * 32 + 4 * j) * 256, kslot + j * 1024);
  };
  auto issue_v = [&](int blk) {
#pragma unroll
    for (int j = 0; j < 8; ++j) dma16<NT_KV>(vs_, vv0 ^ (32 * ((j >> 1) & 1)), (unsigned)(blk * 32 + 8 * (j >> 1)) * 256 + (j & 1) * 128, vslot + j * 1024);
  };
  issue_k(wid); issue_v(wid);
  {
#pragma unroll
    for (int qt = 0; qt < 3; ++qt) {
      u32x2 w2 = {cvtpk(qacc[qt][0] * CSCALE, qacc[qt][1] * CSCALE), cvtpk(qacc[qt][2] * CSCALE, qacc[qt][3] * CSCALE)};
      const int q = 16 * qt + r16, c = 2 * wid + (g >> 1);
      *(LAS u32x2*)(size_t)(ldsb + LDS_QS + q * 256 + 16 * (c ^ r16) + 8 * (g & 1)) = w2;
    }
  }
  BARRIER();
  bf16x8 qf[3][4];
#pragma unroll
  for (int qt = 0; qt < 3; ++qt)
#pragma unroll
    for (int ks = 0; ks < 4; ++ks) qf[qt][ks] = lds_b128(ldsb + LDS_QS + (16 * qt + r16) * 256 + 16 * ((4 * ks + g) ^ r16));

  ST(0,2);
  f32x4 o[8][3];
#pragma unroll
  for (int dt = 0; dt < 8; ++dt)
#pragma unroll
    for (int qt = 0; qt < 3; ++qt) o[dt][qt] = f32x4{0.f, 0.f, 0.f, 0.f};
  float m[3] = {0.f, 0.f, 0.f}, l[3] = {0.f, 0.f, 0.f};
  const unsigned kb0 = kslot + (8 * (r16 >> 2) + (r16 & 3)) * 256 + 16 * (g ^ r16);
  unsigned vb[2][2];
  {
    const int qq = r16 >> 2, p = lane & 3;
#pragma unroll
    for (int t = 0; t < 2; ++t)
#pragma unroll
      for (int e = 0; e < 2; ++e) vb[t][e] = vslot + 2048 * g + 64 * (4 * t + qq) + 16 * ((2 * e + (p >> 1)) ^ ((2 * g + t) & 3)) + 8 * (p & 1);
  }

  for (int t = 0; t < 8; ++t) {
    WAIT_VM(8);
    bf16x8 kf[2][4];
#pragma unroll
    for (int kt = 0; kt < 2; ++kt)
#pragma unroll
      for (int ks = 0; ks < 4; ++ks) kf[kt][ks] = lds_b128((kb0 ^ (64 * ks)) + kt * 1024);
    WAIT_LGKM0();
    if (t + 1 < 8) issue_k(8 * (t + 1) + wid); else asm volatile("s_nop 0" ::: "memory");
    f32x4 s[2][3];
#pragma unroll
    for (int kt = 0; kt < 2; ++kt)
#pragma unroll
      for (int qt = 0; qt < 3; ++qt) {
        f32x4 c = {-m[qt], -m[qt], -m[qt], -m[qt]};
#pragma unroll
        for (int ks = 0; ks < 4; ++ks) c = mfma16(kf[kt][ks], qf[qt][ks], c);
        s[kt][qt] = c;
      }
    float pm[3];
#pragma unroll
    for (int qt = 0; qt < 3; ++qt)
      pm[qt] = fmaxf(fmaxf(fmaxf(s[0][qt][0], s[0][qt][1]), fmaxf(s[0][qt][2], s[0][qt][3])), fmaxf(fmaxf(s[1][qt][0], s[1][qt][1]), fmaxf(s[1][qt][2], s[1][qt][3])));
    const bool need = (t == 0) || (fmaxf(fmaxf(pm[0], pm[1]), pm[2]) > THR);
    if (__any(need)) {
#pragma unroll
      for (int qt = 0; qt < 3; ++qt) {
        float rm = pm[qt];
        rm = fmaxf(rm, __shfl_xor(rm, 16));
        rm = fmaxf(rm, __shfl_xor(rm, 32));
        const float dl = (t == 0) ? rm : fmaxf(rm, 0.f);
        m[qt] += dl;
        const float al = __builtin_amdgcn_exp2f(-dl);
        l[qt] *= al;
#pragma unroll
        for (int kt = 0; kt < 2; ++kt)
#pragma unroll
          for (int e = 0; e < 4; ++e) s[kt][qt][e] -= dl;
        if (t != 0) {
#pragma unroll
          for (int dt = 0; dt < 8; ++dt) o[dt][qt] *= al;
        }
      }
    }
    bf16x8 pf[3];
#pragma unroll
    for (int qt = 0; qt < 3; ++qt) {
      float e[8];
#pragma unroll
      for (int kt = 0; kt < 2; ++kt)
#pragma unroll
        for (int i = 0; i < 4; ++i) e[4 * kt + i] = __builtin_amdgcn_exp2f(s[kt][qt][i]);
      l[qt] += ((e[0] + e[1]) + (e[2] + e[3])) + ((e[4] + e[5]) + (e[6] + e[7]));
      u32x4 w = {cvtpk(e[0], e[1]), cvtpk(e[2], e[3]), cvtpk(e[4], e[5]), cvtpk(e[6], e[7])};
      pf[qt] = __builtin_bit_cast(bf16x8, w);
    }
    if (t + 1 < 8) WAIT_VM(8); else WAIT_VM(0);
    bf16x8 vf[8];
#pragma unroll
    for (int dt = 0; dt < 8; ++dt) vf[dt] = cat(lds_tr(vb[0][dt & 1] + 512 * (dt >> 1)), lds_tr(vb[1][dt & 1] + 512 * (dt >> 1)));
    WAIT_LGKM0();
    if (t + 1 < 8) issue_v(8 * (t + 1) + wid);
#pragma unroll
    for (int dt = 0; dt < 8; ++dt)
#pragma unroll
      for (int qt = 0; qt < 3; ++qt) o[dt][qt] = mfma16(vf[dt], pf[qt], o[dt][qt]);
  }

  ST(0,3);
#pragma unroll
  for (int qt = 0; qt < 3; ++qt) { l[qt] += __shfl_xor(l[qt], 16); l[qt] += __shfl_xor(l[qt], 32); }
  float* stm = (float*)(smem + LDS_ST);
  float* stl = stm + 8 * 48;
  if (g == 0) {
#pragma unroll
    for (int qt = 0; qt < 3; ++qt) { stm[wid * 48 + 16 * qt + r16] = m[qt]; stl[wid * 48 + 16 * qt + r16] = l[qt]; }
  }
  BARRIER();
  float f[3];
#pragma unroll
  for (int qt = 0; qt < 3; ++qt) {
    float M = stm[16 * qt + r16];
#pragma unroll
    for (int w = 1; w < 8; ++w) M = fmaxf(M, stm[w * 48 + 16 * qt + r16]);
    float L = 0.f;
#pragma unroll
    for (int w = 0; w < 8; ++w) L += stl[w * 48 + 16 * qt + r16] * __builtin_amdgcn_exp2f(stm[w * 48 + 16 * qt + r16] - M);
    f[qt] = __builtin_amdgcn_exp2f(m[qt] - M) / L;
  }
#pragma unroll
  for (int dt = 0; dt < 8; ++dt)
#pragma unroll
    for (int qt = 0; qt < 3; ++qt) o[dt][qt] *= f[qt];
  if (wid >= 4) {
#pragma unroll
    for (int dt = 0; dt < 8; ++dt)
#pragma unroll
      for (int qt = 0; qt < 3; ++qt) *(LAS f32x4*)(size_t)(ldsb + (wid - 4) * 24576 + (dt * 3 + qt) * 1024 + lane * 16) = o[dt][qt];
  }
  BARRIER();
  if (wid < 4) {
#pragma unroll
    for (int dt = 0; dt < 8; ++dt)
#pragma unroll
      for (int qt = 0; qt < 3; ++qt) o[dt][qt] += *(const LAS f32x4*)(size_t)(ldsb + wid * 24576 + (dt * 3 + qt) * 1024 + lane * 16);
  }
  BARRIER();
  if (wid < 4) {
#pragma unroll
    for (int dt = 0; dt < 8; ++dt)
#pragma unroll
      for (int qt = 0; qt < 3; ++qt) *(LAS f32x4*)(size_t)(ldsb + wid * 24576 + (dt * 3 + qt) * 1024 + lane * 16) = o[dt][qt];
  }
  BARRIER();
#pragma unroll
  for (int qt = 0; qt < 3; ++qt) {
    f32x4 a = *(const LAS f32x4*)(size_t)(ldsb + (wid * 3 + qt) * 1024 + lane * 16);
#pragma unroll
    for (int w = 1; w < 4; ++w) a += *(const LAS f32x4*)(size_t)(ldsb + w * 24576 + (wid * 3 + qt) * 1024 + lane * 16);
    u32x2 w2 = {cvtpk(a[0], a[1]), cvtpk(a[2], a[3])};
    char* dst = ofrag + ((size_t)(b * 128 + h * 4 + (wid >> 1)) * 3 + qt) * 1024 + (r16 + 16 * (2 * (wid & 1) + (g >> 1))) * 16 + 8 * (g & 1);
    *(u32x2*)dst = w2;
  }
  ST(0,4);
}

__global__ __launch_bounds__(512, 2) void oproj_kernel(const char* __restrict__ ofrag, const char* __restrict__ wo, float* __restrict__ part) {
  extern __shared__ __attribute__((aligned(16))) char smem[];
  const unsigned ldsb = (unsigned)(size_t)smem;
  const int tid = threadIdx.x, lane = tid & 63;
  const int wid = (int)rfl(tid >> 6);
  const int bid = blockIdx.x, xcd = bid & 7, slot = bid >> 3;
  const int kq = xcd >> 1, b = (xcd & 1) * 4 + (slot >> 3), nt = slot & 7;
  ST(1,0);
  {
    srd_t os = mk_srd(ofrag + (size_t)(b * 128 + kq * 32) * 3072, 32 * 3072);
#pragma unroll
    for (int u = 0; u < 12; ++u) {
      const int P = wid * 12 + u;
      dma16<0>(os, lane * 16, P * 1024, ldsb + LDS_X + P * 1024);
    }
  }
  f32x4 acc[3];
  srd_t ws = mk_srd(wo, HQ * DH * DM * 2);
  gemm_phase<1, NT_WO>(acc, ws, (unsigned)(kq * 1024) * (DM * 2) + nt * 256, DM * 2, ldsb, wid, lane);
  const int g = lane >> 4, r16 = lane & 15;
#pragma unroll
  for (int qt = 0; qt < 3; ++qt)
    *(f32x4*)(part + ((size_t)kq * 384 + b * 48 + 16 * qt + r16) * 1024 + nt * 128 + wid * 16 + 4 * g) = acc[qt];
  ST(1,1);
}

__global__ __launch_bounds__(256) void reduce_kernel(const float* __restrict__ part, unsigned* __restrict__ out) {
  const int i = blockIdx.x * 256 + threadIdx.x;
  if (blockIdx.x < 256) ST(2,0);
  f32x4 a = *(const f32x4*)(part + (size_t)i * 4);
#pragma unroll
  for (int k = 1; k < 4; ++k) a += *(const f32x4*)(part + (size_t)k * 384 * 1024 + (size_t)i * 4);
  u32x2 w = {cvtpk(a[0], a[1]), cvtpk(a[2], a[3])};
  *(u32x2*)(out + (size_t)i * 2) = w;
  if (blockIdx.x < 256) ST(2,1);
}

static void launch(void* const* d_in, void* d_out, void* d_ws, hipStream_t stream) {
  (void)hipFuncSetAttribute((const void*)attn_kernel, hipFuncAttributeMaxDynamicSharedMemorySize, LDS_TOTAL);
  (void)hipFuncSetAttribute((const void*)oproj_kernel, hipFuncAttributeMaxDynamicSharedMemorySize, LDS_TOTAL);
  char* ofrag = (char*)d_ws;
  float* part = (float*)((char*)d_ws + (4u << 20));
  attn_kernel<<<256, 512, LDS_TOTAL, stream>>>((const char*)d_in[0], (const char*)d_in[1], (const char*)d_in[2], (const char*)d_in[3], ofrag);
  oproj_kernel<<<256, 512, LDS_TOTAL, stream>>>(ofrag, (const char*)d_in[4], part);
  reduce_kernel<<<384, 256, 0, stream>>>(part, (unsigned*)d_out);
}
}

extern "C" void kernel_launch(void* const* d_in, const int* in_sizes, int n_in, void* d_out, int out_size, void* d_ws, size_t ws_size, hipStream_t stream) {
  k1::launch(d_in, d_out, d_ws, stream);
}
```

```cpp
#include <hip/hip_runtime.h>
#include <hip/hip_bf16.h>
#include <stdint.h>

namespace k1 {
constexpr int B = 8, SQ = 48, DM = 1024, HQ = 32, HKV = 4, SK = 2048, DH = 128;
typedef __bf16 bf16x8 __attribute__((ext_vector_type(8)));
typedef __bf16 bf16x4 __attribute__((ext_vector_type(4)));
typedef __bf16 bf16x2 __attribute__((ext_vector_type(2)));
typedef short s16x4 __attribute__((ext_vector_type(4)));
typedef float f32x4 __attribute__((ext_vector_type(4)));
typedef float f32x2 __attribute__((ext_vector_type(2)));
typedef unsigned u32x4 __attribute__((ext_vector_type(4)));
typedef unsigned u32x2 __attribute__((ext_vector_type(2)));
#define LAS __attribute__((address_space(3)))
#define DI __device__ __forceinline__
typedef __amdgpu_buffer_rsrc_t srd_t;

DI unsigned rfl(unsigned x) { return __builtin_amdgcn_readfirstlane(x); }
DI srd_t mk_srd(const void* p, unsigned bytes) { return __builtin_amdgcn_make_buffer_rsrc((void*)p, (short)0, (int)bytes, 0x00020000); }
template <int NT>
DI void dma16(srd_t srd, unsigned voff, unsigned soff, unsigned ldsaddr) {
  if constexpr (NT) asm volatile("s_mov_b32 m0, %0\n\ts_nop 0\n\tbuffer_load_dwordx4 %1, %2, %3 offen nt lds" ::"s"(ldsaddr), "v"(voff), "s"(srd), "s"(soff) : "memory");
  else asm volatile("s_mov_b32 m0, %0\n\ts_nop 0\n\tbuffer_load_dwordx4 %1, %2, %3 offen lds" ::"s"(ldsaddr), "v"(voff), "s"(srd), "s"(soff) : "memory");
}
#define WAIT_VM(n) asm volatile("s_waitcnt vmcnt(" #n ")" ::: "memory")
#define WAIT_LGKM0() asm volatile("s_waitcnt lgkmcnt(0)" ::: "memory")
#define BARRIER() do { asm volatile("s_waitcnt lgkmcnt(0)" ::: "memory"); __builtin_amdgcn_s_barrier(); asm volatile("" ::: "memory"); } while (0)
DI f32x4 mfma16(bf16x8 a, bf16x8 b, f32x4 c) { return __builtin_amdgcn_mfma_f32_16x16x32_bf16(a, b, c, 0, 0, 0); }
DI bf16x8 lds_b128(unsigned addr) { return *(const LAS bf16x8*)(size_t)addr; }
DI s16x4 lds_tr(unsigned addr) { return __builtin_amdgcn_ds_read_tr16_b64_v4i16((LAS s16x4*)(size_t)addr); }
DI bf16x8 cat(s16x4 lo, s16x4 hi) { return __builtin_bit_cast(bf16x8, __builtin_shufflevector(lo, hi, 0, 1, 2, 3, 4, 5, 6, 7)); }
DI unsigned cvtpk(float lo, float hi) { f32x2 v = {lo, hi}; bf16x2 b = __builtin_convertvector(v, bf16x2); return __builtin_bit_cast(unsigned, b); }

#define ST(kern,i) do{}while(0)
constexpr unsigned LDS_X = 0;
constexpr unsigned LDS_W = 98304;
constexpr unsigned LDS_QS = 131072;
constexpr unsigned LDS_ST = 147456;
constexpr unsigned LDS_TOTAL = 163840;
constexpr float CSCALE = 0.12751743082f;
constexpr float THR = 8.f;

#define NT_KV 1
#define NT_WQ 1
#define NT_WO 1

template <int XMODE, int NTW>
DI void gemm_phase(f32x4 (&acc)[3], srd_t wsrd, unsigned wsoff0, unsigned wrow_stride, unsigned ldsb, int wid, int lane) {
  const int g = lane >> 4, qq = (lane & 15) >> 2, p = lane & 3;
  const unsigned wv = ((lane >> 2) & 7) * wrow_stride + (lane >> 5) * 64 + 16 * ((lane & 3) ^ ((2 * (wid & 1)) ^ ((lane >> 4) & 1)));
  auto issue_w = [&](int kt) {
    const unsigned so = wsoff0 + (unsigned)(kt * 64 + wid * 8) * wrow_stride;
    const unsigned dst = ldsb + LDS_W + (kt & 3) * 16384 + wid * 2048;
    dma16<NTW>(wsrd, wv, so, dst);
    dma16<NTW>(wsrd, wv, so + 128, dst + 1024);
  };
  issue_w(0); issue_w(1); issue_w(2);
  unsigned trb[2];
#pragma unroll
  for (int t = 0; t < 2; ++t) {
    const int ch = 2 * wid + (p >> 1);
    trb[t] = ldsb + LDS_W + 2048 * g + 512 * (ch >> 2) + 64 * (4 * t + qq) + 16 * ((ch & 3) ^ ((2 * g + t) & 3)) + 8 * (p & 1);
  }
  unsigned xb[2];
#pragma unroll
  for (int ks = 0; ks < 2; ++ks) {
    if constexpr (XMODE == 0) { const int row = lane & 15; xb[ks] = ldsb + LDS_X + row * 128 + 16 * ((4 * ks + g) ^ ((row >> 1) & 7)); }
    else xb[ks] = ldsb + LDS_X + ks * 3072 + lane * 16;
  }
#pragma unroll
  for (int qt = 0; qt < 3; ++qt) acc[qt] = f32x4{0.f, 0.f, 0.f, 0.f};
#pragma unroll
  for (int kt = 0; kt < 16; ++kt) {
    if (kt <= 13) WAIT_VM(4); else if (kt == 14) WAIT_VM(2); else WAIT_VM(0);
    BARRIER();
    if (kt + 3 < 16) issue_w(kt + 3);
#pragma unroll
    for (int ks = 0; ks < 2; ++ks) {
      s16x4 lo = lds_tr(trb[0] + (kt & 3) * 16384 + ks * 8192);
      s16x4 hi = lds_tr(trb[1] + (kt & 3) * 16384 + ks * 8192);
      bf16x8 a = cat(lo, hi);
#pragma unroll
      for (int qt = 0; qt < 3; ++qt) {
        bf16x8 b = lds_b128(xb[ks] + kt * 6144 + qt * (XMODE == 0 ? 2048 : 1024));
        acc[qt] = mfma16(a, b, acc[qt]);
      }
    }
  }
}

__global__ __launch_bounds__(512, 2) void attn_kernel(const char* __restrict__ x, const char* __restrict__ kc, const char* __restrict__ vc,
                                                      const char* __restrict__ wq, char* __restrict__ ofrag) {
  extern __shared__ __attribute__((aligned(16))) char smem[];
  const unsigned ldsb = (unsigned)(size_t)smem;
  const int tid = threadIdx.x, lane = tid & 63;
  const int wid = (int)rfl(tid >> 6);
  const int bid = blockIdx.x, xcd = bid & 7, slot = bid >> 3;
  const int kvh = xcd >> 1, b = (xcd & 1) * 4 + (slot >> 3), h = kvh * 8 + (slot & 7);
  const int g = lane >> 4, r16 = lane & 15;

  ST(0,0);
  {
    srd_t xs = mk_srd(x + (size_t)b * SQ * DM * 2, SQ * DM * 2);
#pragma unroll
    for (int u = 0; u < 12; ++u) {
      const int P = wid * 12 + u, kt = P / 6, j = P % 6;
      const unsigned voff = (lane >> 3) * 2048 + 16 * ((lane & 7) ^ ((4 * j + (lane >> 4)) & 7));
      dma16<0>(xs, voff, (unsigned)(j * 8 * 2048 + kt * 128), ldsb + LDS_X + P * 1024);
    }
  }
  f32x4 qacc[3];
  {
    srd_t ws = mk_srd(wq, DM * HQ * DH * 2);
    gemm_phase<0, NT_WQ>(qacc, ws, h * 256, HQ * DH * 2, ldsb, wid, lane);
  }
  BARRIER();
  ST(0,1);
  srd_t ks_ = mk_srd(kc + (size_t)(b * HKV + kvh) * SK * DH * 2, SK * DH * 2);
  srd_t vs_ = mk_srd(vc + (size_t)(b * HKV + kvh) * SK * DH * 2, SK * DH * 2);
  const unsigned kslot = ldsb + wid * 16384, vslot = kslot + 8192;
  const unsigned kv0 = (lane >> 4) * 256 + 16 * ((lane & 15) ^ (lane >> 4));
  const unsigned vv0 = ((lane >> 2) & 7) * 256 + (lane >> 5) * 64 + 16 * ((lane & 3) ^ ((lane >> 4) & 1));
  auto issue_k = [&](int blk) {
#pragma unroll
    for (int j = 0; j < 8; ++j) dma16<NT_KV>(ks_, kv0 ^ (64 * (j >> 1)), (unsigned)(blk * 32 + 4 * j) * 256, kslot + j * 1024);
  };
  auto issue_v = [&](int blk) {
#pragma unroll
    for (int j = 0; j < 8; ++j) dma16<NT_KV>(vs_, vv0 ^ (32 * ((j >> 1) & 1)), (unsigned)(blk * 32 + 8 * (j >> 1)) * 256 + (j & 1) * 128, vslot + j * 1024);
  };
  issue_k(wid); issue_v(wid);
  {
#pragma unroll
    for (int qt = 0; qt < 3; ++qt) {
      u32x2 w2 = {cvtpk(qacc[qt][0] * CSCALE, qacc[qt][1] * CSCALE), cvtpk(qacc[qt][2] * CSCALE, qacc[qt][3] * CSCALE)};
      const int q = 16 * qt + r16, c = 2 * wid + (g >> 1);
      *(LAS u32x2*)(size_t)(ldsb + LDS_QS + q * 256 + 16 * (c ^ r16) + 8 * (g & 1)) = w2;
    }
  }
  BARRIER();
  bf16x8 qf[3][4];
#pragma unroll
  for (int qt = 0; qt < 3; ++qt)
#pragma unroll
    for (int ks = 0; ks < 4; ++ks) qf[qt][ks] = lds_b128(ldsb + LDS_QS + (16 * qt + r16) * 256 + 16 * ((4 * ks + g) ^ r16));

  ST(0,2);
  f32x4 o[8][3];
#pragma unroll
  for (int dt = 0; dt < 8; ++dt)
#pragma unroll
    for (int qt = 0; qt < 3; ++qt) o[dt][qt] = f32x4{0.f, 0.f, 0.f, 0.f};
  float m[3] = {0.f, 0.f, 0.f}, l[3] = {0.f, 0.f, 0.f};
  const unsigned kb0 = kslot + (8 * (r16 >> 2) + (r16 & 3)) * 256 + 16 * (g ^ r16);
  unsigned vb[2][2];
  {
    const int qq = r16 >> 2, p = lane & 3;
#pragma unroll
    for (int t = 0; t < 2; ++t)
#pragma unroll
      for (int e = 0; e < 2; ++e) vb[t][e] = vslot + 2048 * g + 64 * (4 * t + qq) + 16 * ((2 * e + (p >> 1)) ^ ((2 * g + t) & 3)) + 8 * (p & 1);
  }

  for (int t = 0; t < 8; ++t) {
    WAIT_VM(8);
    bf16x8 kf[2][4];
#pragma unroll
    for (int kt = 0; kt < 2; ++kt)
#pragma unroll
      for (int ks = 0; ks < 4; ++ks) kf[kt][ks] = lds_b128((kb0 ^ (64 * ks)) + kt * 1024);
    WAIT_LGKM0();
    if (t + 1 < 8) issue_k(8 * (t + 1) + wid); else asm volatile("s_nop 0" ::: "memory");
    f32x4 s[2][3];
#pragma unroll
    for (int kt = 0; kt < 2; ++kt)
#pragma unroll
      for (int qt = 0; qt < 3; ++qt) {
        f32x4 c = {-m[qt], -m[qt], -m[qt], -m[qt]};
#pragma unroll
        for (int ks = 0; ks < 4; ++ks) c = mfma16(kf[kt][ks], qf[qt][ks], c);
        s[kt][qt] = c;
      }
    float pm[3];
#pragma unroll
    for (int qt = 0; qt < 3; ++qt)
      pm[qt] = fmaxf(fmaxf(fmaxf(s[0][qt][0], s[0][qt][1]), fmaxf(s[0][qt][2], s[0][qt][3])), fmaxf(fmaxf(s[1][qt][0], s[1][qt][1]), fmaxf(s[1][qt][2], s[1][qt][3])));
    const bool need = (t == 0) || (fmaxf(fmaxf(pm[0], pm[1]), pm[2]) > THR);
    if (__any(need)) {
#pragma unroll
      for (int qt = 0; qt < 3; ++qt) {
        float rm = pm[qt];
        rm = fmaxf(rm, __shfl_xor(rm, 16));
        rm = fmaxf(rm, __shfl_xor(rm, 32));
        const float dl = (t == 0) ? rm : fmaxf(rm, 0.f);
        m[qt] += dl;
        const float al = __builtin_amdgcn_exp2f(-dl);
        l[qt] *= al;
#pragma unroll
        for (int kt = 0; kt < 2; ++kt)
#pragma unroll
          for (int e = 0; e < 4; ++e) s[kt][qt][e] -= dl;
        if (t != 0) {
#pragma unroll
          for (int dt = 0; dt < 8; ++dt) o[dt][qt] *= al;
        }
      }
    }
    bf16x8 pf[3];
#pragma unroll
    for (int qt = 0; qt < 3; ++qt) {
      float e[8];
#pragma unroll
      for (int kt = 0; kt < 2; ++kt)
#pragma unroll
        for (int i = 0; i < 4; ++i) e[4 * kt + i] = __builtin_amdgcn_exp2f(s[kt][qt][i]);
      l[qt] += ((e[0] + e[1]) + (e[2] + e[3])) + ((e[4] + e[5]) + (e[6] + e[7]));
      u32x4 w = {cvtpk(e[0], e[1]), cvtpk(e[2], e[3]), cvtpk(e[4], e[5]), cvtpk(e[6], e[7])};
      pf[qt] = __builtin_bit_cast(bf16x8, w);
    }
    if (t + 1 < 8) WAIT_VM(8); else WAIT_VM(0);
    bf16x8 vf[8];
#pragma unroll
    for (int dt = 0; dt < 8; ++dt) vf[dt] = cat(lds_tr(vb[0][dt & 1] + 512 * (dt >> 1)), lds_tr(vb[1][dt & 1] + 512 * (dt >> 1)));
    WAIT_LGKM0();
    if (t + 1 < 8) issue_v(8 * (t + 1) + wid);
#pragma unroll
    for (int dt = 0; dt < 8; ++dt)
#pragma unroll
      for (int qt = 0; qt < 3; ++qt) o[dt][qt] = mfma16(vf[dt], pf[qt], o[dt][qt]);
  }

  ST(0,3);
#pragma unroll
  for (int qt = 0; qt < 3; ++qt) { l[qt] += __shfl_xor(l[qt], 16); l[qt] += __shfl_xor(l[qt], 32); }
  float* stm = (float*)(smem + LDS_ST);
  float* stl = stm + 8 * 48;
  if (g == 0) {
#pragma unroll
    for (int qt = 0; qt < 3; ++qt) { stm[wid * 48 + 16 * qt + r16] = m[qt]; stl[wid * 48 + 16 * qt + r16] = l[qt]; }
  }
  BARRIER();
  float f[3];
#pragma unroll
  for (int qt = 0; qt < 3; ++qt) {
    float M = stm[16 * qt + r16];
#pragma unroll
    for (int w = 1; w < 8; ++w) M = fmaxf(M, stm[w * 48 + 16 * qt + r16]);
    float L = 0.f;
#pragma unroll
    for (int w = 0; w < 8; ++w) L += stl[w * 48 + 16 * qt + r16] * __builtin_amdgcn_exp2f(stm[w * 48 + 16 * qt + r16] - M);
    f[qt] = __builtin_amdgcn_exp2f(m[qt] - M) / L;
  }
#pragma unroll
  for (int dt = 0; dt < 8; ++dt)
#pragma unroll
    for (int qt = 0; qt < 3; ++qt) o[dt][qt] *= f[qt];
  if (wid >= 4) {
#pragma unroll
    for (int dt = 0; dt < 8; ++dt)
#pragma unroll
      for (int qt = 0; qt < 3; ++qt) *(LAS f32x4*)(size_t)(ldsb + (wid - 4) * 24576 + (dt * 3 + qt) * 1024 + lane * 16) = o[dt][qt];
  }
  BARRIER();
  if (wid < 4) {
#pragma unroll
    for (int dt = 0; dt < 8; ++dt)
#pragma unroll
      for (int qt = 0; qt < 3; ++qt) o[dt][qt] += *(const LAS f32x4*)(size_t)(ldsb + wid * 24576 + (dt * 3 + qt) * 1024 + lane * 16);
  }
  BARRIER();
  if (wid < 4) {
#pragma unroll
    for (int dt = 0; dt < 8; ++dt)
#pragma unroll
      for (int qt = 0; qt < 3; ++qt) *(LAS f32x4*)(size_t)(ldsb + wid * 24576 + (dt * 3 + qt) * 1024 + lane * 16) = o[dt][qt];
  }
  BARRIER();
#pragma unroll
  for (int qt = 0; qt < 3; ++qt) {
    f32x4 a = *(const LAS f32x4*)(size_t)(ldsb + (wid * 3 + qt) * 1024 + lane * 16);
#pragma unroll
    for (int w = 1; w < 4; ++w) a += *(const LAS f32x4*)(size_t)(ldsb + w * 24576 + (wid * 3 + qt) * 1024 + lane * 16);
    u32x2 w2 = {cvtpk(a[0], a[1]), cvtpk(a[2], a[3])};
    char* dst = ofrag + ((size_t)(b * 128 + h * 4 + (wid >> 1)) * 3 + qt) * 1024 + (r16 + 16 * (2 * (wid & 1) + (g >> 1))) * 16 + 8 * (g & 1);
    *(u32x2*)dst = w2;
  }
  ST(0,4);
}

__global__ __launch_bounds__(512, 2) void oproj_kernel(const char* __restrict__ ofrag, const char* __restrict__ wo, float* __restrict__ part) {
  extern __shared__ __attribute__((aligned(16))) char smem[];
  const unsigned ldsb = (unsigned)(size_t)smem;
  const int tid = threadIdx.x, lane = tid & 63;
  const int wid = (int)rfl(tid >> 6);
  const int bid = blockIdx.x, xcd = bid & 7, slot = bid >> 3;
  const int kq = xcd >> 1, b = (xcd & 1) * 4 + (slot >> 3), nt = slot & 7;
  ST(1,0);
  {
    srd_t os = mk_srd(ofrag + (size_t)(b * 128 + kq * 32) * 3072, 32 * 3072);
#pragma unroll
    for (int u = 0; u < 12; ++u) {
      const int P = wid * 12 + u;
      dma16<0>(os, lane * 16, P * 1024, ldsb + LDS_X + P * 1024);
    }
  }
  f32x4 acc[3];
  srd_t ws = mk_srd(wo, HQ * DH * DM * 2);
  gemm_phase<1, NT_WO>(acc, ws, (unsigned)(kq * 1024) * (DM * 2) + nt * 256, DM * 2, ldsb, wid, lane);
  const int g = lane >> 4, r16 = lane & 15;
#pragma unroll
  for (int qt = 0; qt < 3; ++qt)
    *(f32x4*)(part + ((size_t)kq * 384 + b * 48 + 16 * qt + r16) * 1024 + nt * 128 + wid * 16 + 4 * g) = acc[qt];
  ST(1,1);
}

__global__ __launch_bounds__(256) void reduce_kernel(const float* __restrict__ part, unsigned* __restrict__ out) {
  const int i = blockIdx.x * 256 + threadIdx.x;
  if (blockIdx.x < 256) ST(2,0);
  f32x4 a = *(const f32x4*)(part + (size_t)i * 4);
#pragma unroll
  for (int k = 1; k < 4; ++k) a += *(const f32x4*)(part + (size_t)k * 384 * 1024 + (size_t)i * 4);
  u32x2 w = {cvtpk(a[0], a[1]), cvtpk(a[2], a[3])};
  *(u32x2*)(out + (size_t)i * 2) = w;
  if (blockIdx.x < 256) ST(2,1);
}

static void launch(void* const* d_in, void* d_out, void* d_ws, hipStream_t stream) {
  (void)hipFuncSetAttribute((const void*)attn_kernel, hipFuncAttributeMaxDynamicSharedMemorySize, LDS_TOTAL);
  (void)hipFuncSetAttribute((const void*)oproj_kernel, hipFuncAttributeMaxDynamicSharedMemorySize, LDS_TOTAL);
  char* ofrag = (char*)d_ws;
  float* part = (float*)((char*)d_ws + (4u << 20));
  attn_kernel<<<256, 512, LDS_TOTAL, stream>>>((const char*)d_in[0], (const char*)d_in[1], (const char*)d_in[2], (const char*)d_in[3], ofrag);
  oproj_kernel<<<256, 512, LDS_TOTAL, stream>>>(ofrag, (const char*)d_in[4], part);
  reduce_kernel<<<384, 256, 0, stream>>>(part, (unsigned*)d_out);
}
}

extern "C" void kernel_launch(void* const* d_in, const int* in_sizes, int n_in, void* d_out, int out_size, void* d_ws, size_t ws_size, hipStream_t stream) {
  k1::launch(d_in, d_out, d_ws, stream);
}
```

```cpp
#include <hip/hip_runtime.h>
#include <hip/hip_bf16.h>
#include <stdint.h>

namespace k1 {
constexpr int B = 8, SQ = 48, DM = 1024, HQ = 32, HKV = 4, SK = 2048, DH = 128;
typedef __bf16 bf16x2 __attribute__((ext_vector_type(2)));
typedef float f32x4 __attribute__((ext_vector_type(4)));
typedef float f32x2 __attribute__((ext_vector_type(2)));
typedef float f32x8 __attribute__((ext_vector_type(8)));
typedef float f32x16 __attribute__((ext_vector_type(16)));
typedef float f32x32 __attribute__((ext_vector_type(32)));
typedef unsigned u32x4 __attribute__((ext_vector_type(4)));
typedef unsigned u32x2 __attribute__((ext_vector_type(2)));
typedef unsigned long long u64;
#define LAS __attribute__((address_space(3)))
#define GAS __attribute__((address_space(1)))
#define DI __device__ __forceinline__
typedef __amdgpu_buffer_rsrc_t srd_t;

DI unsigned rfl(unsigned x) { return __builtin_amdgcn_readfirstlane(x); }
DI srd_t mk_srd(const void* p, unsigned bytes) { return __builtin_amdgcn_make_buffer_rsrc((void*)p, (short)0, (int)bytes, 0x00020000); }
template <bool SC1 = false>
DI void dma16(srd_t srd, unsigned voff, unsigned soff, unsigned ldsaddr) {
  if constexpr (SC1) asm volatile("s_mov_b32 m0, %0\n\ts_nop 0\n\tbuffer_load_dwordx4 %1, %2, %3 offen sc1 lds" ::"s"(ldsaddr), "v"(voff), "s"(srd), "s"(soff) : "memory");
  else asm volatile("s_mov_b32 m0, %0\n\ts_nop 0\n\tbuffer_load_dwordx4 %1, %2, %3 offen lds" ::"s"(ldsaddr), "v"(voff), "s"(srd), "s"(soff) : "memory");
}
constexpr unsigned OOB = 0x7fffffffu;
DI void touch(srd_t srd, unsigned voff, unsigned soff) { asm volatile("buffer_load_dword v255, %0, %1, %2 offen" ::"v"(voff), "s"(srd), "s"(soff) : "memory", "v255"); }
template <int N> DI void wait_vm() { asm volatile("s_waitcnt vmcnt(%0)" ::"i"(N) : "memory"); }
#define BARRIER() asm volatile("s_waitcnt lgkmcnt(0)\n\ts_barrier" ::: "memory")
#define BARE_BARRIER() asm volatile("s_barrier" ::: "memory")
DI unsigned cvtpk(float lo, float hi) { f32x2 v = {lo, hi}; bf16x2 b = __builtin_convertvector(v, bf16x2); return __builtin_bit_cast(unsigned, b); }
DI u64 ld_flag(const u64* p) { return __hip_atomic_load((const GAS u64*)p, __ATOMIC_RELAXED, __HIP_MEMORY_SCOPE_AGENT); }
DI void st_flag(u64* p, u64 v) { __hip_atomic_store((GAS u64*)p, v, __ATOMIC_RELAXED, __HIP_MEMORY_SCOPE_AGENT); }
DI void poll_flags(const u64* p, int n, u64 tag, int lane) {
  for (unsigned spins = 0; spins < (1u << 18); ++spins) {
    const u64 v = lane < n ? ld_flag(p + lane) : tag;
    if (__all(v == tag)) break;
    __builtin_amdgcn_s_sleep(1);
  }
  asm volatile("" ::: "memory");
}

#define ATTN_LOOP_ASM \
  "ds_read_b128 v[96:99], v176 offset:0\n\t" \
  "ds_read_b128 v[100:103], v177 offset:0\n\t" \
  "ds_read_b128 v[104:107], v178 offset:0\n\t" \
  "ds_read_b128 v[108:111], v179 offset:0\n\t" \
  "ds_read_b128 v[112:115], v176 offset:4096\n\t" \
  "ds_read_b128 v[116:119], v177 offset:4096\n\t" \
  "ds_read_b128 v[120:123], v178 offset:4096\n\t" \
  "ds_read_b128 v[124:127], v179 offset:4096\n\t" \
  "ds_read_b128 v[128:131], v176 offset:8192\n\t" \
  "ds_read_b128 v[132:135], v177 offset:8192\n\t" \
  "ds_read_b128 v[136:139], v178 offset:8192\n\t" \
  "ds_read_b128 v[140:143], v179 offset:8192\n\t" \
  "v_mov_b64 v[0:1], 0\n\t" \
  "v_mov_b64 v[2:3], 0\n\t" \
  "v_mov_b64 v[4:5], 0\n\t" \
  "v_mov_b64 v[6:7], 0\n\t" \
  "v_mov_b64 v[8:9], 0\n\t" \
  "v_mov_b64 v[10:11], 0\n\t" \
  "v_mov_b64 v[12:13], 0\n\t" \
  "v_mov_b64 v[14:15], 0\n\t" \
  "v_mov_b64 v[16:17], 0\n\t" \
  "v_mov_b64 v[18:19], 0\n\t" \
  "v_mov_b64 v[20:21], 0\n\t" \
  "v_mov_b64 v[22:23], 0\n\t" \
  "v_mov_b64 v[24:25], 0\n\t" \
  "v_mov_b64 v[26:27], 0\n\t" \
  "v_mov_b64 v[28:29], 0\n\t" \
  "v_mov_b64 v[30:31], 0\n\t" \
  "v_mov_b64 v[32:33], 0\n\t" \
  "v_mov_b64 v[34:35], 0\n\t" \
  "v_mov_b64 v[36:37], 0\n\t" \
  "v_mov_b64 v[38:39], 0\n\t" \
  "v_mov_b64 v[40:41], 0\n\t" \
  "v_mov_b64 v[42:43], 0\n\t" \
  "v_mov_b64 v[44:45], 0\n\t" \
  "v_mov_b64 v[46:47], 0\n\t" \
  "v_mov_b64 v[48:49], 0\n\t" \
  "v_mov_b64 v[50:51], 0\n\t" \
  "v_mov_b64 v[52:53], 0\n\t" \
  "v_mov_b64 v[54:55], 0\n\t" \
  "v_mov_b64 v[56:57], 0\n\t" \
  "v_mov_b64 v[58:59], 0\n\t" \
  "v_mov_b64 v[60:61], 0\n\t" \
  "v_mov_b64 v[62:63], 0\n\t" \
  "v_mov_b64 v[64:65], 0\n\t" \
  "v_mov_b64 v[66:67], 0\n\t" \
  "v_mov_b64 v[68:69], 0\n\t" \
  "v_mov_b64 v[70:71], 0\n\t" \
  "v_mov_b64 v[72:73], 0\n\t" \
  "v_mov_b64 v[74:75], 0\n\t" \
  "v_mov_b64 v[76:77], 0\n\t" \
  "v_mov_b64 v[78:79], 0\n\t" \
  "v_mov_b64 v[80:81], 0\n\t" \
  "v_mov_b64 v[82:83], 0\n\t" \
  "v_mov_b64 v[84:85], 0\n\t" \
  "v_mov_b64 v[86:87], 0\n\t" \
  "v_mov_b64 v[88:89], 0\n\t" \
  "v_mov_b64 v[90:91], 0\n\t" \
  "v_mov_b64 v[92:93], 0\n\t" \
  "v_mov_b64 v[94:95], 0\n\t" \
  "v_mov_b64 v[200:201], 0\n\t" \
  "v_mov_b64 v[202:203], 0\n\t" \
  "v_mov_b64 v[204:205], 0\n\t" \
  "v_mov_b64 v[206:207], 0\n\t" \
  "v_mov_b64 v[208:209], 0\n\t" \
  "v_mov_b64 v[210:211], 0\n\t" \
  "v_mov_b64 v[212:213], 0\n\t" \
  "v_mov_b64 v[214:215], 0\n\t" \
  "v_mov_b64 v[216:217], 0\n\t" \
  "v_mov_b64 v[218:219], 0\n\t" \
  "v_mov_b64 v[220:221], 0\n\t" \
  "v_mov_b64 v[222:223], 0\n\t" \
  "v_mov_b32 v224, 0x3f803f80\n\t" \
  "v_mov_b32 v225, 0x3f803f80\n\t" \
  "v_mov_b32 v226, 0x3f803f80\n\t" \
  "v_mov_b32 v227, 0x3f803f80\n\t" \
  "s_waitcnt lgkmcnt(0)\n\t" \
  "s_barrier\n\t" \
  "ds_read_b128 v[144:147], v228 offset:0\n\t" \
  "ds_read_b128 v[148:151], v228 offset:1024\n\t" \
  "ds_read_b128 v[152:155], v229 offset:0\n\t" \
  "ds_read_b128 v[156:159], v229 offset:1024\n\t" \
  "ds_read_b128 v[160:163], v230 offset:0\n\t" \
  "ds_read_b128 v[164:167], v230 offset:1024\n\t" \
  "ds_read_b128 v[168:171], v231 offset:0\n\t" \
  "ds_read_b128 v[172:175], v231 offset:1024\n\t" \
  "LOOP_%=:\n\t" \
  "s_waitcnt lgkmcnt(7)\n\t" \
  "v_mfma_f32_16x16x32_bf16 v[176:179], v[144:147], v[96:99], v[200:203]\n\t" \
  "s_waitcnt lgkmcnt(6)\n\t" \
  "v_mfma_f32_16x16x32_bf16 v[180:183], v[148:151], v[96:99], v[200:203]\n\t" \
  "s_waitcnt lgkmcnt(5)\n\t" \
  "v_mfma_f32_16x16x32_bf16 v[176:179], v[152:155], v[100:103], v[176:179]\n\t" \
  "s_waitcnt lgkmcnt(4)\n\t" \
  "v_mfma_f32_16x16x32_bf16 v[180:183], v[156:159], v[100:103], v[180:183]\n\t" \
  "s_waitcnt lgkmcnt(3)\n\t" \
  "v_mfma_f32_16x16x32_bf16 v[176:179], v[160:163], v[104:107], v[176:179]\n\t" \
  "s_waitcnt lgkmcnt(2)\n\t" \
  "v_mfma_f32_16x16x32_bf16 v[180:183], v[164:167], v[104:107], v[180:183]\n\t" \
  "v_add_u32 v228, %[dk], v228\n\t" \
  "v_add_u32 v229, %[dk], v229\n\t" \
  "s_waitcnt lgkmcnt(1)\n\t" \
  "v_mfma_f32_16x16x32_bf16 v[176:179], v[168:171], v[108:111], v[176:179]\n\t" \
  "v_add_u32 v230, %[dk], v230\n\t" \
  "v_add_u32 v231, %[dk], v231\n\t" \
  "s_waitcnt lgkmcnt(0)\n\t" \
  "v_mfma_f32_16x16x32_bf16 v[180:183], v[172:175], v[108:111], v[180:183]\n\t" \
  "v_add_u32 v232, %[dv], v232\n\t" \
  "v_add_u32 v233, %[dv], v233\n\t" \
  "v_mfma_f32_16x16x32_bf16 v[184:187], v[144:147], v[112:115], v[204:207]\n\t" \
  "v_add_u32 v234, %[dv], v234\n\t" \
  "v_add_u32 v235, %[dv], v235\n\t" \
  "v_mfma_f32_16x16x32_bf16 v[188:191], v[148:151], v[112:115], v[204:207]\n\t" \
  "v_max3_f32 v236, v176, v177, v178\n\t" \
  "v_mfma_f32_16x16x32_bf16 v[184:187], v[152:155], v[116:119], v[184:187]\n\t" \
  "v_max3_f32 v237, v179, v180, v181\n\t" \
  "v_max3_f32 v236, v236, v182, v183\n\t" \
  "v_mfma_f32_16x16x32_bf16 v[188:191], v[156:159], v[116:119], v[188:191]\n\t" \
  "v_max_f32 v236, v236, v237\n\t" \
  "v_cmp_lt_f32 vcc, %[thr], v236\n\t" \
  "v_mfma_f32_16x16x32_bf16 v[184:187], v[160:163], v[120:123], v[184:187]\n\t" \
  "s_cbranch_vccnz RARE0_%=\n\t" \
  "BACK0_%=:\n\t" \
  "v_exp_f32 v176, v176\n\t" \
  "v_mfma_f32_16x16x32_bf16 v[188:191], v[164:167], v[120:123], v[188:191]\n\t" \
  "v_exp_f32 v177, v177\n\t" \
  "v_exp_f32 v178, v178\n\t" \
  "v_mfma_f32_16x16x32_bf16 v[184:187], v[168:171], v[124:127], v[184:187]\n\t" \
  "v_exp_f32 v179, v179\n\t" \
  "v_cvt_pk_bf16_f32 v176, v176, v177\n\t" \
  "v_mfma_f32_16x16x32_bf16 v[188:191], v[172:175], v[124:127], v[188:191]\n\t" \
  "v_exp_f32 v180, v180\n\t" \
  "v_cvt_pk_bf16_f32 v177, v178, v179\n\t" \
  "s_barrier\n\t" \
  "v_mfma_f32_16x16x32_bf16 v[192:195], v[144:147], v[128:131], v[208:211]\n\t" \
  "ds_read_b64_tr_b16 v[144:145], v232 offset:0\n\t" \
  "ds_read_b64_tr_b16 v[146:147], v234 offset:0\n\t" \
  "v_exp_f32 v181, v181\n\t" \
  "v_mfma_f32_16x16x32_bf16 v[196:199], v[148:151], v[128:131], v[208:211]\n\t" \
  "ds_read_b64_tr_b16 v[148:149], v233 offset:0\n\t" \
  "ds_read_b64_tr_b16 v[150:151], v235 offset:0\n\t" \
  "v_exp_f32 v182, v182\n\t" \
  "v_mfma_f32_16x16x32_bf16 v[192:195], v[152:155], v[132:135], v[192:195]\n\t" \
  "ds_read_b64_tr_b16 v[152:153], v232 offset:512\n\t" \
  "ds_read_b64_tr_b16 v[154:155], v234 offset:512\n\t" \
  "v_exp_f32 v183, v183\n\t" \
  "v_mfma_f32_16x16x32_bf16 v[196:199], v[156:159], v[132:135], v[196:199]\n\t" \
  "ds_read_b64_tr_b16 v[156:157], v233 offset:512\n\t" \
  "ds_read_b64_tr_b16 v[158:159], v235 offset:512\n\t" \
  "v_cvt_pk_bf16_f32 v178, v180, v181\n\t" \
  "v_max3_f32 v236, v184, v185, v186\n\t" \
  "v_mfma_f32_16x16x32_bf16 v[192:195], v[160:163], v[136:139], v[192:195]\n\t" \
  "ds_read_b64_tr_b16 v[160:161], v232 offset:1024\n\t" \
  "ds_read_b64_tr_b16 v[162:163], v234 offset:1024\n\t" \
  "v_cvt_pk_bf16_f32 v179, v182, v183\n\t" \
  "v_max3_f32 v237, v187, v188, v189\n\t" \
  "v_mfma_f32_16x16x32_bf16 v[196:199], v[164:167], v[136:139], v[196:199]\n\t" \
  "ds_read_b64_tr_b16 v[164:165], v233 offset:1024\n\t" \
  "ds_read_b64_tr_b16 v[166:167], v235 offset:1024\n\t" \
  "v_max3_f32 v236, v236, v190, v191\n\t" \
  "v_max_f32 v236, v236, v237\n\t" \
  "v_mfma_f32_16x16x32_bf16 v[192:195], v[168:171], v[140:143], v[192:195]\n\t" \
  "ds_read_b64_tr_b16 v[168:169], v232 offset:1536\n\t" \
  "ds_read_b64_tr_b16 v[170:171], v234 offset:1536\n\t" \
  "v_cmp_lt_f32 vcc, %[thr], v236\n\t" \
  "v_mfma_f32_16x16x32_bf16 v[196:199], v[172:175], v[140:143], v[196:199]\n\t" \
  "ds_read_b64_tr_b16 v[172:173], v233 offset:1536\n\t" \
  "ds_read_b64_tr_b16 v[174:175], v235 offset:1536\n\t" \
  "s_waitcnt lgkmcnt(14)\n\t" \
  "v_mfma_f32_16x16x32_bf16 v[0:3], v[144:147], v[176:179], v[0:3]\n\t" \
  "s_cbranch_vccnz RARE1_%=\n\t" \
  "BACK1_%=:\n\t" \
  "v_exp_f32 v184, v184\n\t" \
  "s_waitcnt lgkmcnt(12)\n\t" \
  "v_mfma_f32_16x16x32_bf16 v[4:7], v[148:151], v[176:179], v[4:7]\n\t" \
  "v_exp_f32 v185, v185\n\t" \
  "v_max3_f32 v236, v192, v193, v194\n\t" \
  "s_waitcnt lgkmcnt(10)\n\t" \
  "v_mfma_f32_16x16x32_bf16 v[8:11], v[152:155], v[176:179], v[8:11]\n\t" \
  "v_exp_f32 v186, v186\n\t" \
  "v_max3_f32 v237, v195, v196, v197\n\t" \
  "s_waitcnt lgkmcnt(8)\n\t" \
  "v_mfma_f32_16x16x32_bf16 v[12:15], v[156:159], v[176:179], v[12:15]\n\t" \
  "v_exp_f32 v187, v187\n\t" \
  "v_cvt_pk_bf16_f32 v184, v184, v185\n\t" \
  "s_waitcnt lgkmcnt(6)\n\t" \
  "v_mfma_f32_16x16x32_bf16 v[16:19], v[160:163], v[176:179], v[16:19]\n\t" \
  "v_exp_f32 v188, v188\n\t" \
  "v_cvt_pk_bf16_f32 v185, v186, v187\n\t" \
  "s_waitcnt lgkmcnt(4)\n\t" \
  "v_mfma_f32_16x16x32_bf16 v[20:23], v[164:167], v[176:179], v[20:23]\n\t" \
  "v_exp_f32 v189, v189\n\t" \
  "v_max3_f32 v236, v236, v198, v199\n\t" \
  "s_waitcnt lgkmcnt(2)\n\t" \
  "v_mfma_f32_16x16x32_bf16 v[24:27], v[168:171], v[176:179], v[24:27]\n\t" \
  "v_exp_f32 v190, v190\n\t" \
  "v_max_f32 v236, v236, v237\n\t" \
  "s_waitcnt lgkmcnt(0)\n\t" \
  "v_mfma_f32_16x16x32_bf16 v[28:31], v[172:175], v[176:179], v[28:31]\n\t" \
  "v_exp_f32 v191, v191\n\t" \
  "v_cvt_pk_bf16_f32 v186, v188, v189\n\t" \
  "v_mfma_f32_16x16x32_bf16 v[212:215], v[224:227], v[176:179], v[212:215]\n\t" \
  "v_cvt_pk_bf16_f32 v187, v190, v191\n\t" \
  "v_cmp_lt_f32 vcc, %[thr], v236\n\t" \
  "s_nop 0\n\t" \
  "v_mfma_f32_16x16x32_bf16 v[32:35], v[144:147], v[184:187], v[32:35]\n\t" \
  "s_cbranch_vccnz RARE2_%=\n\t" \
  "BACK2_%=:\n\t" \
  "v_exp_f32 v192, v192\n\t" \
  "v_mfma_f32_16x16x32_bf16 v[36:39], v[148:151], v[184:187], v[36:39]\n\t" \
  "v_exp_f32 v193, v193\n\t" \
  "v_mfma_f32_16x16x32_bf16 v[40:43], v[152:155], v[184:187], v[40:43]\n\t" \
  "v_exp_f32 v194, v194\n\t" \
  "v_cvt_pk_bf16_f32 v192, v192, v193\n\t" \
  "v_mfma_f32_16x16x32_bf16 v[44:47], v[156:159], v[184:187], v[44:47]\n\t" \
  "v_exp_f32 v195, v195\n\t" \
  "v_mfma_f32_16x16x32_bf16 v[48:51], v[160:163], v[184:187], v[48:51]\n\t" \
  "v_exp_f32 v196, v196\n\t" \
  "v_cvt_pk_bf16_f32 v193, v194, v195\n\t" \
  "v_mfma_f32_16x16x32_bf16 v[52:55], v[164:167], v[184:187], v[52:55]\n\t" \
  "v_exp_f32 v197, v197\n\t" \
  "v_mfma_f32_16x16x32_bf16 v[56:59], v[168:171], v[184:187], v[56:59]\n\t" \
  "v_exp_f32 v198, v198\n\t" \
  "v_cvt_pk_bf16_f32 v194, v196, v197\n\t" \
  "v_mfma_f32_16x16x32_bf16 v[60:63], v[172:175], v[184:187], v[60:63]\n\t" \
  "v_exp_f32 v199, v199\n\t" \
  "v_mfma_f32_16x16x32_bf16 v[216:219], v[224:227], v[184:187], v[216:219]\n\t" \
  "v_cvt_pk_bf16_f32 v195, v198, v199\n\t" \
  "s_sub_u32 %[cnt], %[cnt], 1\n\t" \
  "s_cmp_eq_u32 %[cnt], 0\n\t" \
  "s_cbranch_scc1 TAIL_%=\n\t" \
  "s_barrier\n\t" \
  "s_nop 0\n\t" \
  "v_mfma_f32_16x16x32_bf16 v[64:67], v[144:147], v[192:195], v[64:67]\n\t" \
  "ds_read_b128 v[144:147], v228 offset:0\n\t" \
  "v_mfma_f32_16x16x32_bf16 v[68:71], v[148:151], v[192:195], v[68:71]\n\t" \
  "ds_read_b128 v[148:151], v228 offset:1024\n\t" \
  "v_mfma_f32_16x16x32_bf16 v[72:75], v[152:155], v[192:195], v[72:75]\n\t" \
  "ds_read_b128 v[152:155], v229 offset:0\n\t" \
  "v_mfma_f32_16x16x32_bf16 v[76:79], v[156:159], v[192:195], v[76:79]\n\t" \
  "ds_read_b128 v[156:159], v229 offset:1024\n\t" \
  "v_mfma_f32_16x16x32_bf16 v[80:83], v[160:163], v[192:195], v[80:83]\n\t" \
  "ds_read_b128 v[160:163], v230 offset:0\n\t" \
  "v_mfma_f32_16x16x32_bf16 v[84:87], v[164:167], v[192:195], v[84:87]\n\t" \
  "ds_read_b128 v[164:167], v230 offset:1024\n\t" \
  "v_mfma_f32_16x16x32_bf16 v[88:91], v[168:171], v[192:195], v[88:91]\n\t" \
  "ds_read_b128 v[168:171], v231 offset:0\n\t" \
  "v_mfma_f32_16x16x32_bf16 v[92:95], v[172:175], v[192:195], v[92:95]\n\t" \
  "ds_read_b128 v[172:175], v231 offset:1024\n\t" \
  "v_mfma_f32_16x16x32_bf16 v[220:223], v[224:227], v[192:195], v[220:223]\n\t" \
  "s_mov_b32 %[dv], %[dk]\n\t" \
  "s_sub_u32 %[dk], 0, %[dk]\n\t" \
  "s_mov_b32 %[thr], 0x41000000\n\t" \
  "s_mov_b32 %[flo], 0\n\t" \
  "s_branch LOOP_%=\n\t" \
  "TAIL_%=:\n\t" \
  "s_nop 1\n\t" \
  "v_mfma_f32_16x16x32_bf16 v[64:67], v[144:147], v[192:195], v[64:67]\n\t" \
  "v_mfma_f32_16x16x32_bf16 v[68:71], v[148:151], v[192:195], v[68:71]\n\t" \
  "v_mfma_f32_16x16x32_bf16 v[72:75], v[152:155], v[192:195], v[72:75]\n\t" \
  "v_mfma_f32_16x16x32_bf16 v[76:79], v[156:159], v[192:195], v[76:79]\n\t" \
  "v_mfma_f32_16x16x32_bf16 v[80:83], v[160:163], v[192:195], v[80:83]\n\t" \
  "v_mfma_f32_16x16x32_bf16 v[84:87], v[164:167], v[192:195], v[84:87]\n\t" \
  "v_mfma_f32_16x16x32_bf16 v[88:91], v[168:171], v[192:195], v[88:91]\n\t" \
  "v_mfma_f32_16x16x32_bf16 v[92:95], v[172:175], v[192:195], v[92:95]\n\t" \
  "v_mfma_f32_16x16x32_bf16 v[220:223], v[224:227], v[192:195], v[220:223]\n\t" \
  "s_nop 7\n\t" \
  "s_nop 3\n\t" \
  "s_branch END_%=\n\t" \
  "RARE0_%=:\n\t" \
  "s_nop 7\n\t" \
  "v_mov_b32 v237, v236\n\t" \
  "v_mov_b32 v238, v236\n\t" \
  "s_nop 1\n\t" \
  "v_permlane16_swap_b32 v237, v238\n\t" \
  "s_nop 1\n\t" \
  "v_max_f32 v236, v237, v238\n\t" \
  "v_mov_b32 v237, v236\n\t" \
  "v_mov_b32 v238, v236\n\t" \
  "s_nop 1\n\t" \
  "v_permlane32_swap_b32 v237, v238\n\t" \
  "s_nop 1\n\t" \
  "v_max_f32 v236, v237, v238\n\t" \
  "v_max_f32 v237, %[flo], v236\n\t" \
  "v_max_f32 v238, 0, v237\n\t" \
  "v_exp_f32 v239, -v238\n\t" \
  "v_sub_f32 v200, v200, v237\n\t" \
  "v_sub_f32 v201, v201, v237\n\t" \
  "v_sub_f32 v202, v202, v237\n\t" \
  "v_sub_f32 v203, v203, v237\n\t" \
  "v_sub_f32 v176, v176, v237\n\t" \
  "v_sub_f32 v177, v177, v237\n\t" \
  "v_sub_f32 v178, v178, v237\n\t" \
  "v_sub_f32 v179, v179, v237\n\t" \
  "v_sub_f32 v180, v180, v237\n\t" \
  "v_sub_f32 v181, v181, v237\n\t" \
  "v_sub_f32 v182, v182, v237\n\t" \
  "v_sub_f32 v183, v183, v237\n\t" \
  "s_cmp_lg_u32 %[flo], 0\n\t" \
  "s_cbranch_scc1 NOMUL0_%=\n\t" \
  "v_mul_f32 v212, v239, v212\n\t" \
  "v_mul_f32 v213, v239, v213\n\t" \
  "v_mul_f32 v214, v239, v214\n\t" \
  "v_mul_f32 v215, v239, v215\n\t" \
  "v_mul_f32 v0, v239, v0\n\t" \
  "v_mul_f32 v1, v239, v1\n\t" \
  "v_mul_f32 v2, v239, v2\n\t" \
  "v_mul_f32 v3, v239, v3\n\t" \
  "v_mul_f32 v4, v239, v4\n\t" \
  "v_mul_f32 v5, v239, v5\n\t" \
  "v_mul_f32 v6, v239, v6\n\t" \
  "v_mul_f32 v7, v239, v7\n\t" \
  "v_mul_f32 v8, v239, v8\n\t" \
  "v_mul_f32 v9, v239, v9\n\t" \
  "v_mul_f32 v10, v239, v10\n\t" \
  "v_mul_f32 v11, v239, v11\n\t" \
  "v_mul_f32 v12, v239, v12\n\t" \
  "v_mul_f32 v13, v239, v13\n\t" \
  "v_mul_f32 v14, v239, v14\n\t" \
  "v_mul_f32 v15, v239, v15\n\t" \
  "v_mul_f32 v16, v239, v16\n\t" \
  "v_mul_f32 v17, v239, v17\n\t" \
  "v_mul_f32 v18, v239, v18\n\t" \
  "v_mul_f32 v19, v239, v19\n\t" \
  "v_mul_f32 v20, v239, v20\n\t" \
  "v_mul_f32 v21, v239, v21\n\t" \
  "v_mul_f32 v22, v239, v22\n\t" \
  "v_mul_f32 v23, v239, v23\n\t" \
  "v_mul_f32 v24, v239, v24\n\t" \
  "v_mul_f32 v25, v239, v25\n\t" \
  "v_mul_f32 v26, v239, v26\n\t" \
  "v_mul_f32 v27, v239, v27\n\t" \
  "v_mul_f32 v28, v239, v28\n\t" \
  "v_mul_f32 v29, v239, v29\n\t" \
  "v_mul_f32 v30, v239, v30\n\t" \
  "v_mul_f32 v31, v239, v31\n\t" \
  "NOMUL0_%=:\n\t" \
  "s_nop 1\n\t" \
  "s_branch BACK0_%=\n\t" \
  "RARE1_%=:\n\t" \
  "s_nop 7\n\t" \
  "v_mov_b32 v237, v236\n\t" \
  "v_mov_b32 v238, v236\n\t" \
  "s_nop 1\n\t" \
  "v_permlane16_swap_b32 v237, v238\n\t" \
  "s_nop 1\n\t" \
  "v_max_f32 v236, v237, v238\n\t" \
  "v_mov_b32 v237, v236\n\t" \
  "v_mov_b32 v238, v236\n\t" \
  "s_nop 1\n\t" \
  "v_permlane32_swap_b32 v237, v238\n\t" \
  "s_nop 1\n\t" \
  "v_max_f32 v236, v237, v238\n\t" \
  "v_max_f32 v237, %[flo], v236\n\t" \
  "v_max_f32 v238, 0, v237\n\t" \
  "v_exp_f32 v239, -v238\n\t" \
  "v_sub_f32 v204, v204, v237\n\t" \
  "v_sub_f32 v205, v205, v237\n\t" \
  "v_sub_f32 v206, v206, v237\n\t" \
  "v_sub_f32 v207, v207, v237\n\t" \
  "v_sub_f32 v184, v184, v237\n\t" \
  "v_sub_f32 v185, v185, v237\n\t" \
  "v_sub_f32 v186, v186, v237\n\t" \
  "v_sub_f32 v187, v187, v237\n\t" \
  "v_sub_f32 v188, v188, v237\n\t" \
  "v_sub_f32 v189, v189, v237\n\t" \
  "v_sub_f32 v190, v190, v237\n\t" \
  "v_sub_f32 v191, v191, v237\n\t" \
  "s_cmp_lg_u32 %[flo], 0\n\t" \
  "s_cbranch_scc1 NOMUL1_%=\n\t" \
  "v_mul_f32 v216, v239, v216\n\t" \
  "v_mul_f32 v217, v239, v217\n\t" \
  "v_mul_f32 v218, v239, v218\n\t" \
  "v_mul_f32 v219, v239, v219\n\t" \
  "v_mul_f32 v32, v239, v32\n\t" \
  "v_mul_f32 v33, v239, v33\n\t" \
  "v_mul_f32 v34, v239, v34\n\t" \
  "v_mul_f32 v35, v239, v35\n\t" \
  "v_mul_f32 v36, v239, v36\n\t" \
  "v_mul_f32 v37, v239, v37\n\t" \
  "v_mul_f32 v38, v239, v38\n\t" \
  "v_mul_f32 v39, v239, v39\n\t" \
  "v_mul_f32 v40, v239, v40\n\t" \
  "v_mul_f32 v41, v239, v41\n\t" \
  "v_mul_f32 v42, v239, v42\n\t" \
  "v_mul_f32 v43, v239, v43\n\t" \
  "v_mul_f32 v44, v239, v44\n\t" \
  "v_mul_f32 v45, v239, v45\n\t" \
  "v_mul_f32 v46, v239, v46\n\t" \
  "v_mul_f32 v47, v239, v47\n\t" \
  "v_mul_f32 v48, v239, v48\n\t" \
  "v_mul_f32 v49, v239, v49\n\t" \
  "v_mul_f32 v50, v239, v50\n\t" \
  "v_mul_f32 v51, v239, v51\n\t" \
  "v_mul_f32 v52, v239, v52\n\t" \
  "v_mul_f32 v53, v239, v53\n\t" \
  "v_mul_f32 v54, v239, v54\n\t" \
  "v_mul_f32 v55, v239, v55\n\t" \
  "v_mul_f32 v56, v239, v56\n\t" \
  "v_mul_f32 v57, v239, v57\n\t" \
  "v_mul_f32 v58, v239, v58\n\t" \
  "v_mul_f32 v59, v239, v59\n\t" \
  "v_mul_f32 v60, v239, v60\n\t" \
  "v_mul_f32 v61, v239, v61\n\t" \
  "v_mul_f32 v62, v239, v62\n\t" \
  "v_mul_f32 v63, v239, v63\n\t" \
  "NOMUL1_%=:\n\t" \
  "s_nop 1\n\t" \
  "s_branch BACK1_%=\n\t" \
  "RARE2_%=:\n\t" \
  "s_nop 7\n\t" \
  "v_mov_b32 v237, v236\n\t" \
  "v_mov_b32 v238, v236\n\t" \
  "s_nop 1\n\t" \
  "v_permlane16_swap_b32 v237, v238\n\t" \
  "s_nop 1\n\t" \
  "v_max_f32 v236, v237, v238\n\t" \
  "v_mov_b32 v237, v236\n\t" \
  "v_mov_b32 v238, v236\n\t" \
  "s_nop 1\n\t" \
  "v_permlane32_swap_b32 v237, v238\n\t" \
  "s_nop 1\n\t" \
  "v_max_f32 v236, v237, v238\n\t" \
  "v_max_f32 v237, %[flo], v236\n\t" \
  "v_max_f32 v238, 0, v237\n\t" \
  "v_exp_f32 v239, -v238\n\t" \
  "v_sub_f32 v208, v208, v237\n\t" \
  "v_sub_f32 v209, v209, v237\n\t" \
  "v_sub_f32 v210, v210, v237\n\t" \
  "v_sub_f32 v211, v211, v237\n\t" \
  "v_sub_f32 v192, v192, v237\n\t" \
  "v_sub_f32 v193, v193, v237\n\t" \
  "v_sub_f32 v194, v194, v237\n\t" \
  "v_sub_f32 v195, v195, v237\n\t" \
  "v_sub_f32 v196, v196, v237\n\t" \
  "v_sub_f32 v197, v197, v237\n\t" \
  "v_sub_f32 v198, v198, v237\n\t" \
  "v_sub_f32 v199, v199, v237\n\t" \
  "s_cmp_lg_u32 %[flo], 0\n\t" \
  "s_cbranch_scc1 NOMUL2_%=\n\t" \
  "v_mul_f32 v220, v239, v220\n\t" \
  "v_mul_f32 v221, v239, v221\n\t" \
  "v_mul_f32 v222, v239, v222\n\t" \
  "v_mul_f32 v223, v239, v223\n\t" \
  "v_mul_f32 v64, v239, v64\n\t" \
  "v_mul_f32 v65, v239, v65\n\t" \
  "v_mul_f32 v66, v239, v66\n\t" \
  "v_mul_f32 v67, v239, v67\n\t" \
  "v_mul_f32 v68, v239, v68\n\t" \
  "v_mul_f32 v69, v239, v69\n\t" \
  "v_mul_f32 v70, v239, v70\n\t" \
  "v_mul_f32 v71, v239, v71\n\t" \
  "v_mul_f32 v72, v239, v72\n\t" \
  "v_mul_f32 v73, v239, v73\n\t" \
  "v_mul_f32 v74, v239, v74\n\t" \
  "v_mul_f32 v75, v239, v75\n\t" \
  "v_mul_f32 v76, v239, v76\n\t" \
  "v_mul_f32 v77, v239, v77\n\t" \
  "v_mul_f32 v78, v239, v78\n\t" \
  "v_mul_f32 v79, v239, v79\n\t" \
  "v_mul_f32 v80, v239, v80\n\t" \
  "v_mul_f32 v81, v239, v81\n\t" \
  "v_mul_f32 v82, v239, v82\n\t" \
  "v_mul_f32 v83, v239, v83\n\t" \
  "v_mul_f32 v84, v239, v84\n\t" \
  "v_mul_f32 v85, v239, v85\n\t" \
  "v_mul_f32 v86, v239, v86\n\t" \
  "v_mul_f32 v87, v239, v87\n\t" \
  "v_mul_f32 v88, v239, v88\n\t" \
  "v_mul_f32 v89, v239, v89\n\t" \
  "v_mul_f32 v90, v239, v90\n\t" \
  "v_mul_f32 v91, v239, v91\n\t" \
  "v_mul_f32 v92, v239, v92\n\t" \
  "v_mul_f32 v93, v239, v93\n\t" \
  "v_mul_f32 v94, v239, v94\n\t" \
  "v_mul_f32 v95, v239, v95\n\t" \
  "NOMUL2_%=:\n\t" \
  "s_nop 1\n\t" \
  "s_branch BACK2_%=\n\t" \
  "END_%=:\n\t"
#define ATTN_LOOP_CLOBBERS "v96", "v97", "v98", "v99", "v100", "v101", "v102", "v103", "v104", "v105", "v106", "v107", "v108", "v109", "v110", "v111", "v112", "v113", "v114", "v115", "v116", "v117", "v118", "v119", "v120", "v121", "v122", "v123", "v124", "v125", "v126", "v127", "v128", "v129", "v130", "v131", "v132", "v133", "v134", "v135", "v136", "v137", "v138", "v139", "v140", "v141", "v142", "v143", "v144", "v145", "v146", "v147", "v148", "v149", "v150", "v151", "v152", "v153", "v154", "v155", "v156", "v157", "v158", "v159", "v160", "v161", "v162", "v163", "v164", "v165", "v166", "v167", "v168", "v169", "v170", "v171", "v172", "v173", "v174", "v175", "v180", "v181", "v182", "v183", "v184", "v185", "v186", "v187", "v188", "v189", "v190", "v191", "v192", "v193", "v194", "v195", "v196", "v197", "v198", "v199", "v201", "v202", "v203", "v205", "v206", "v207", "v209", "v210", "v211", "v213", "v214", "v215", "v217", "v218", "v219", "v221", "v222", "v223", "v224", "v225", "v226", "v227", "v236", "v237", "v238", "v239"

#define GEMM_Q_ASM \
  "s_barrier\n\t" \
  "ds_read_b64_tr_b16 v[24:25], v104 offset:0\n\t" \
  "ds_read_b64_tr_b16 v[26:27], v105 offset:0\n\t" \
  "ds_read_b64_tr_b16 v[28:29], v106 offset:0\n\t" \
  "ds_read_b64_tr_b16 v[30:31], v107 offset:0\n\t" \
  "ds_read_b128 v[56:59], v108 offset:0\n\t" \
  "ds_read_b128 v[60:63], v108 offset:2048\n\t" \
  "ds_read_b128 v[64:67], v108 offset:4096\n\t" \
  "ds_read_b64_tr_b16 v[32:33], v104 offset:8192\n\t" \
  "ds_read_b64_tr_b16 v[34:35], v105 offset:8192\n\t" \
  "ds_read_b64_tr_b16 v[36:37], v106 offset:8192\n\t" \
  "ds_read_b64_tr_b16 v[38:39], v107 offset:8192\n\t" \
  "ds_read_b128 v[68:71], v109 offset:0\n\t" \
  "ds_read_b128 v[72:75], v109 offset:2048\n\t" \
  "ds_read_b128 v[76:79], v109 offset:4096\n\t" \
  "s_waitcnt lgkmcnt(0)\n\t" \
  "s_barrier\n\t" \
  "v_mfma_f32_16x16x32_bf16 v[0:3], v[24:27], v[56:59], 0\n\t" \
  "ds_read_b64_tr_b16 v[40:41], v104 offset:16384\n\t" \
  "ds_read_b64_tr_b16 v[42:43], v105 offset:16384\n\t" \
  "v_mfma_f32_16x16x32_bf16 v[4:7], v[24:27], v[60:63], 0\n\t" \
  "ds_read_b64_tr_b16 v[44:45], v106 offset:16384\n\t" \
  "ds_read_b64_tr_b16 v[46:47], v107 offset:16384\n\t" \
  "v_mfma_f32_16x16x32_bf16 v[8:11], v[24:27], v[64:67], 0\n\t" \
  "ds_read_b128 v[80:83], v108 offset:6144\n\t" \
  "ds_read_b128 v[84:87], v108 offset:8192\n\t" \
  "v_mfma_f32_16x16x32_bf16 v[12:15], v[28:31], v[56:59], 0\n\t" \
  "ds_read_b128 v[88:91], v108 offset:10240\n\t" \
  "ds_read_b64_tr_b16 v[48:49], v104 offset:24576\n\t" \
  "v_mfma_f32_16x16x32_bf16 v[16:19], v[28:31], v[60:63], 0\n\t" \
  "ds_read_b64_tr_b16 v[50:51], v105 offset:24576\n\t" \
  "ds_read_b64_tr_b16 v[52:53], v106 offset:24576\n\t" \
  "v_mfma_f32_16x16x32_bf16 v[20:23], v[28:31], v[64:67], 0\n\t" \
  "ds_read_b64_tr_b16 v[54:55], v107 offset:24576\n\t" \
  "ds_read_b128 v[92:95], v109 offset:6144\n\t" \
  "v_mfma_f32_16x16x32_bf16 v[0:3], v[32:35], v[68:71], v[0:3]\n\t" \
  "ds_read_b128 v[96:99], v109 offset:8192\n\t" \
  "ds_read_b128 v[100:103], v109 offset:10240\n\t" \
  "v_mfma_f32_16x16x32_bf16 v[4:7], v[32:35], v[72:75], v[4:7]\n\t" \
  "v_mfma_f32_16x16x32_bf16 v[8:11], v[32:35], v[76:79], v[8:11]\n\t" \
  "v_mfma_f32_16x16x32_bf16 v[12:15], v[36:39], v[68:71], v[12:15]\n\t" \
  "v_mfma_f32_16x16x32_bf16 v[16:19], v[36:39], v[72:75], v[16:19]\n\t" \
  "v_mfma_f32_16x16x32_bf16 v[20:23], v[36:39], v[76:79], v[20:23]\n\t" \
  "s_waitcnt lgkmcnt(0)\n\t" \
  "s_barrier\n\t" \
  "v_mfma_f32_16x16x32_bf16 v[0:3], v[40:43], v[80:83], v[0:3]\n\t" \
  "ds_read_b64_tr_b16 v[24:25], v104 offset:32768\n\t" \
  "ds_read_b64_tr_b16 v[26:27], v105 offset:32768\n\t" \
  "v_mfma_f32_16x16x32_bf16 v[4:7], v[40:43], v[84:87], v[4:7]\n\t" \
  "ds_read_b64_tr_b16 v[28:29], v106 offset:32768\n\t" \
  "ds_read_b64_tr_b16 v[30:31], v107 offset:32768\n\t" \
  "v_mfma_f32_16x16x32_bf16 v[8:11], v[40:43], v[88:91], v[8:11]\n\t" \
  "ds_read_b128 v[56:59], v108 offset:12288\n\t" \
  "ds_read_b128 v[60:63], v108 offset:14336\n\t" \
  "v_mfma_f32_16x16x32_bf16 v[12:15], v[44:47], v[80:83], v[12:15]\n\t" \
  "ds_read_b128 v[64:67], v108 offset:16384\n\t" \
  "ds_read_b64_tr_b16 v[32:33], v104 offset:40960\n\t" \
  "v_mfma_f32_16x16x32_bf16 v[16:19], v[44:47], v[84:87], v[16:19]\n\t" \
  "ds_read_b64_tr_b16 v[34:35], v105 offset:40960\n\t" \
  "ds_read_b64_tr_b16 v[36:37], v106 offset:40960\n\t" \
  "v_mfma_f32_16x16x32_bf16 v[20:23], v[44:47], v[88:91], v[20:23]\n\t" \
  "ds_read_b64_tr_b16 v[38:39], v107 offset:40960\n\t" \
  "ds_read_b128 v[68:71], v109 offset:12288\n\t" \
  "v_mfma_f32_16x16x32_bf16 v[0:3], v[48:51], v[92:95], v[0:3]\n\t" \
  "ds_read_b128 v[72:75], v109 offset:14336\n\t" \
  "ds_read_b128 v[76:79], v109 offset:16384\n\t" \
  "v_mfma_f32_16x16x32_bf16 v[4:7], v[48:51], v[96:99], v[4:7]\n\t" \
  "v_mfma_f32_16x16x32_bf16 v[8:11], v[48:51], v[100:103], v[8:11]\n\t" \
  "v_mfma_f32_16x16x32_bf16 v[12:15], v[52:55], v[92:95], v[12:15]\n\t" \
  "v_mfma_f32_16x16x32_bf16 v[16:19], v[52:55], v[96:99], v[16:19]\n\t" \
  "v_mfma_f32_16x16x32_bf16 v[20:23], v[52:55], v[100:103], v[20:23]\n\t" \
  "s_waitcnt lgkmcnt(0)\n\t" \
  "s_barrier\n\t" \
  "v_mfma_f32_16x16x32_bf16 v[0:3], v[24:27], v[56:59], v[0:3]\n\t" \
  "ds_read_b64_tr_b16 v[40:41], v104 offset:49152\n\t" \
  "ds_read_b64_tr_b16 v[42:43], v105 offset:49152\n\t" \
  "v_mfma_f32_16x16x32_bf16 v[4:7], v[24:27], v[60:63], v[4:7]\n\t" \
  "ds_read_b64_tr_b16 v[44:45], v106 offset:49152\n\t" \
  "ds_read_b64_tr_b16 v[46:47], v107 offset:49152\n\t" \
  "v_mfma_f32_16x16x32_bf16 v[8:11], v[24:27], v[64:67], v[8:11]\n\t" \
  "ds_read_b128 v[80:83], v108 offset:18432\n\t" \
  "ds_read_b128 v[84:87], v108 offset:20480\n\t" \
  "v_mfma_f32_16x16x32_bf16 v[12:15], v[28:31], v[56:59], v[12:15]\n\t" \
  "ds_read_b128 v[88:91], v108 offset:22528\n\t" \
  "ds_read_b64_tr_b16 v[48:49], v104 offset:57344\n\t" \
  "v_mfma_f32_16x16x32_bf16 v[16:19], v[28:31], v[60:63], v[16:19]\n\t" \
  "ds_read_b64_tr_b16 v[50:51], v105 offset:57344\n\t" \
  "ds_read_b64_tr_b16 v[52:53], v106 offset:57344\n\t" \
  "v_mfma_f32_16x16x32_bf16 v[20:23], v[28:31], v[64:67], v[20:23]\n\t" \
  "ds_read_b64_tr_b16 v[54:55], v107 offset:57344\n\t" \
  "ds_read_b128 v[92:95], v109 offset:18432\n\t" \
  "v_mfma_f32_16x16x32_bf16 v[0:3], v[32:35], v[68:71], v[0:3]\n\t" \
  "ds_read_b128 v[96:99], v109 offset:20480\n\t" \
  "ds_read_b128 v[100:103], v109 offset:22528\n\t" \
  "v_mfma_f32_16x16x32_bf16 v[4:7], v[32:35], v[72:75], v[4:7]\n\t" \
  "v_mfma_f32_16x16x32_bf16 v[8:11], v[32:35], v[76:79], v[8:11]\n\t" \
  "v_mfma_f32_16x16x32_bf16 v[12:15], v[36:39], v[68:71], v[12:15]\n\t" \
  "v_mfma_f32_16x16x32_bf16 v[16:19], v[36:39], v[72:75], v[16:19]\n\t" \
  "v_mfma_f32_16x16x32_bf16 v[20:23], v[36:39], v[76:79], v[20:23]\n\t" \
  "s_waitcnt lgkmcnt(0)\n\t" \
  "s_barrier\n\t" \
  "v_mfma_f32_16x16x32_bf16 v[0:3], v[40:43], v[80:83], v[0:3]\n\t" \
  "ds_read_b64_tr_b16 v[24:25], v104 offset:0\n\t" \
  "ds_read_b64_tr_b16 v[26:27], v105 offset:0\n\t" \
  "v_mfma_f32_16x16x32_bf16 v[4:7], v[40:43], v[84:87], v[4:7]\n\t" \
  "ds_read_b64_tr_b16 v[28:29], v106 offset:0\n\t" \
  "ds_read_b64_tr_b16 v[30:31], v107 offset:0\n\t" \
  "v_mfma_f32_16x16x32_bf16 v[8:11], v[40:43], v[88:91], v[8:11]\n\t" \
  "ds_read_b128 v[56:59], v108 offset:24576\n\t" \
  "ds_read_b128 v[60:63], v108 offset:26624\n\t" \
  "v_mfma_f32_16x16x32_bf16 v[12:15], v[44:47], v[80:83], v[12:15]\n\t" \
  "ds_read_b128 v[64:67], v108 offset:28672\n\t" \
  "ds_read_b64_tr_b16 v[32:33], v104 offset:8192\n\t" \
  "v_mfma_f32_16x16x32_bf16 v[16:19], v[44:47], v[84:87], v[16:19]\n\t" \
  "ds_read_b64_tr_b16 v[34:35], v105 offset:8192\n\t" \
  "ds_read_b64_tr_b16 v[36:37], v106 offset:8192\n\t" \
  "v_mfma_f32_16x16x32_bf16 v[20:23], v[44:47], v[88:91], v[20:23]\n\t" \
  "ds_read_b64_tr_b16 v[38:39], v107 offset:8192\n\t" \
  "ds_read_b128 v[68:71], v109 offset:24576\n\t" \
  "v_mfma_f32_16x16x32_bf16 v[0:3], v[48:51], v[92:95], v[0:3]\n\t" \
  "ds_read_b128 v[72:75], v109 offset:26624\n\t" \
  "ds_read_b128 v[76:79], v109 offset:28672\n\t" \
  "v_mfma_f32_16x16x32_bf16 v[4:7], v[48:51], v[96:99], v[4:7]\n\t" \
  "v_mfma_f32_16x16x32_bf16 v[8:11], v[48:51], v[100:103], v[8:11]\n\t" \
  "v_mfma_f32_16x16x32_bf16 v[12:15], v[52:55], v[92:95], v[12:15]\n\t" \
  "v_mfma_f32_16x16x32_bf16 v[16:19], v[52:55], v[96:99], v[16:19]\n\t" \
  "v_mfma_f32_16x16x32_bf16 v[20:23], v[52:55], v[100:103], v[20:23]\n\t" \
  "s_waitcnt lgkmcnt(0)\n\t" \
  "s_barrier\n\t" \
  "v_mfma_f32_16x16x32_bf16 v[0:3], v[24:27], v[56:59], v[0:3]\n\t" \
  "ds_read_b64_tr_b16 v[40:41], v104 offset:16384\n\t" \
  "ds_read_b64_tr_b16 v[42:43], v105 offset:16384\n\t" \
  "v_mfma_f32_16x16x32_bf16 v[4:7], v[24:27], v[60:63], v[4:7]\n\t" \
  "ds_read_b64_tr_b16 v[44:45], v106 offset:16384\n\t" \
  "ds_read_b64_tr_b16 v[46:47], v107 offset:16384\n\t" \
  "v_mfma_f32_16x16x32_bf16 v[8:11], v[24:27], v[64:67], v[8:11]\n\t" \
  "ds_read_b128 v[80:83], v108 offset:30720\n\t" \
  "ds_read_b128 v[84:87], v108 offset:32768\n\t" \
  "v_mfma_f32_16x16x32_bf16 v[12:15], v[28:31], v[56:59], v[12:15]\n\t" \
  "ds_read_b128 v[88:91], v108 offset:34816\n\t" \
  "ds_read_b64_tr_b16 v[48:49], v104 offset:24576\n\t" \
  "v_mfma_f32_16x16x32_bf16 v[16:19], v[28:31], v[60:63], v[16:19]\n\t" \
  "ds_read_b64_tr_b16 v[50:51], v105 offset:24576\n\t" \
  "ds_read_b64_tr_b16 v[52:53], v106 offset:24576\n\t" \
  "v_mfma_f32_16x16x32_bf16 v[20:23], v[28:31], v[64:67], v[20:23]\n\t" \
  "ds_read_b64_tr_b16 v[54:55], v107 offset:24576\n\t" \
  "ds_read_b128 v[92:95], v109 offset:30720\n\t" \
  "v_mfma_f32_16x16x32_bf16 v[0:3], v[32:35], v[68:71], v[0:3]\n\t" \
  "ds_read_b128 v[96:99], v109 offset:32768\n\t" \
  "ds_read_b128 v[100:103], v109 offset:34816\n\t" \
  "v_mfma_f32_16x16x32_bf16 v[4:7], v[32:35], v[72:75], v[4:7]\n\t" \
  "v_mfma_f32_16x16x32_bf16 v[8:11], v[32:35], v[76:79], v[8:11]\n\t" \
  "v_mfma_f32_16x16x32_bf16 v[12:15], v[36:39], v[68:71], v[12:15]\n\t" \
  "v_mfma_f32_16x16x32_bf16 v[16:19], v[36:39], v[72:75], v[16:19]\n\t" \
  "v_mfma_f32_16x16x32_bf16 v[20:23], v[36:39], v[76:79], v[20:23]\n\t" \
  "s_waitcnt lgkmcnt(0)\n\t" \
  "s_barrier\n\t" \
  "v_mfma_f32_16x16x32_bf16 v[0:3], v[40:43], v[80:83], v[0:3]\n\t" \
  "ds_read_b64_tr_b16 v[24:25], v104 offset:32768\n\t" \
  "ds_read_b64_tr_b16 v[26:27], v105 offset:32768\n\t" \
  "v_mfma_f32_16x16x32_bf16 v[4:7], v[40:43], v[84:87], v[4:7]\n\t" \
  "ds_read_b64_tr_b16 v[28:29], v106 offset:32768\n\t" \
  "ds_read_b64_tr_b16 v[30:31], v107 offset:32768\n\t" \
  "v_mfma_f32_16x16x32_bf16 v[8:11], v[40:43], v[88:91], v[8:11]\n\t" \
  "ds_read_b128 v[56:59], v108 offset:36864\n\t" \
  "ds_read_b128 v[60:63], v108 offset:38912\n\t" \
  "v_mfma_f32_16x16x32_bf16 v[12:15], v[44:47], v[80:83], v[12:15]\n\t" \
  "ds_read_b128 v[64:67], v108 offset:40960\n\t" \
  "ds_read_b64_tr_b16 v[32:33], v104 offset:40960\n\t" \
  "v_mfma_f32_16x16x32_bf16 v[16:19], v[44:47], v[84:87], v[16:19]\n\t" \
  "ds_read_b64_tr_b16 v[34:35], v105 offset:40960\n\t" \
  "ds_read_b64_tr_b16 v[36:37], v106 offset:40960\n\t" \
  "v_mfma_f32_16x16x32_bf16 v[20:23], v[44:47], v[88:91], v[20:23]\n\t" \
  "ds_read_b64_tr_b16 v[38:39], v107 offset:40960\n\t" \
  "ds_read_b128 v[68:71], v109 offset:36864\n\t" \
  "v_mfma_f32_16x16x32_bf16 v[0:3], v[48:51], v[92:95], v[0:3]\n\t" \
  "ds_read_b128 v[72:75], v109 offset:38912\n\t" \
  "ds_read_b128 v[76:79], v109 offset:40960\n\t" \
  "v_mfma_f32_16x16x32_bf16 v[4:7], v[48:51], v[96:99], v[4:7]\n\t" \
  "v_mfma_f32_16x16x32_bf16 v[8:11], v[48:51], v[100:103], v[8:11]\n\t" \
  "v_mfma_f32_16x16x32_bf16 v[12:15], v[52:55], v[92:95], v[12:15]\n\t" \
  "v_mfma_f32_16x16x32_bf16 v[16:19], v[52:55], v[96:99], v[16:19]\n\t" \
  "v_mfma_f32_16x16x32_bf16 v[20:23], v[52:55], v[100:103], v[20:23]\n\t" \
  "s_waitcnt lgkmcnt(0)\n\t" \
  "s_barrier\n\t" \
  "v_mfma_f32_16x16x32_bf16 v[0:3], v[24:27], v[56:59], v[0:3]\n\t" \
  "ds_read_b64_tr_b16 v[40:41], v104 offset:49152\n\t" \
  "ds_read_b64_tr_b16 v[42:43], v105 offset:49152\n\t" \
  "v_mfma_f32_16x16x32_bf16 v[4:7], v[24:27], v[60:63], v[4:7]\n\t" \
  "ds_read_b64_tr_b16 v[44:45], v106 offset:49152\n\t" \
  "ds_read_b64_tr_b16 v[46:47], v107 offset:49152\n\t" \
  "v_mfma_f32_16x16x32_bf16 v[8:11], v[24:27], v[64:67], v[8:11]\n\t" \
  "ds_read_b128 v[80:83], v108 offset:43008\n\t" \
  "ds_read_b128 v[84:87], v108 offset:45056\n\t" \
  "v_mfma_f32_16x16x32_bf16 v[12:15], v[28:31], v[56:59], v[12:15]\n\t" \
  "ds_read_b128 v[88:91], v108 offset:47104\n\t" \
  "ds_read_b64_tr_b16 v[48:49], v104 offset:57344\n\t" \
  "v_mfma_f32_16x16x32_bf16 v[16:19], v[28:31], v[60:63], v[16:19]\n\t" \
  "ds_read_b64_tr_b16 v[50:51], v105 offset:57344\n\t" \
  "ds_read_b64_tr_b16 v[52:53], v106 offset:57344\n\t" \
  "v_mfma_f32_16x16x32_bf16 v[20:23], v[28:31], v[64:67], v[20:23]\n\t" \
  "ds_read_b64_tr_b16 v[54:55], v107 offset:57344\n\t" \
  "ds_read_b128 v[92:95], v109 offset:43008\n\t" \
  "v_mfma_f32_16x16x32_bf16 v[0:3], v[32:35], v[68:71], v[0:3]\n\t" \
  "ds_read_b128 v[96:99], v109 offset:45056\n\t" \
  "ds_read_b128 v[100:103], v109 offset:47104\n\t" \
  "v_mfma_f32_16x16x32_bf16 v[4:7], v[32:35], v[72:75], v[4:7]\n\t" \
  "v_mfma_f32_16x16x32_bf16 v[8:11], v[32:35], v[76:79], v[8:11]\n\t" \
  "v_mfma_f32_16x16x32_bf16 v[12:15], v[36:39], v[68:71], v[12:15]\n\t" \
  "v_mfma_f32_16x16x32_bf16 v[16:19], v[36:39], v[72:75], v[16:19]\n\t" \
  "v_mfma_f32_16x16x32_bf16 v[20:23], v[36:39], v[76:79], v[20:23]\n\t" \
  "s_waitcnt lgkmcnt(0)\n\t" \
  "s_barrier\n\t" \
  "v_mfma_f32_16x16x32_bf16 v[0:3], v[40:43], v[80:83], v[0:3]\n\t" \
  "ds_read_b64_tr_b16 v[24:25], v104 offset:0\n\t" \
  "ds_read_b64_tr_b16 v[26:27], v105 offset:0\n\t" \
  "v_mfma_f32_16x16x32_bf16 v[4:7], v[40:43], v[84:87], v[4:7]\n\t" \
  "ds_read_b64_tr_b16 v[28:29], v106 offset:0\n\t" \
  "ds_read_b64_tr_b16 v[30:31], v107 offset:0\n\t" \
  "v_mfma_f32_16x16x32_bf16 v[8:11], v[40:43], v[88:91], v[8:11]\n\t" \
  "ds_read_b128 v[56:59], v110 offset:0\n\t" \
  "ds_read_b128 v[60:63], v110 offset:2048\n\t" \
  "v_mfma_f32_16x16x32_bf16 v[12:15], v[44:47], v[80:83], v[12:15]\n\t" \
  "ds_read_b128 v[64:67], v110 offset:4096\n\t" \
  "ds_read_b64_tr_b16 v[32:33], v104 offset:8192\n\t" \
  "v_mfma_f32_16x16x32_bf16 v[16:19], v[44:47], v[84:87], v[16:19]\n\t" \
  "ds_read_b64_tr_b16 v[34:35], v105 offset:8192\n\t" \
  "ds_read_b64_tr_b16 v[36:37], v106 offset:8192\n\t" \
  "v_mfma_f32_16x16x32_bf16 v[20:23], v[44:47], v[88:91], v[20:23]\n\t" \
  "ds_read_b64_tr_b16 v[38:39], v107 offset:8192\n\t" \
  "ds_read_b128 v[68:71], v111 offset:0\n\t" \
  "v_mfma_f32_16x16x32_bf16 v[0:3], v[48:51], v[92:95], v[0:3]\n\t" \
  "ds_read_b128 v[72:75], v111 offset:2048\n\t" \
  "ds_read_b128 v[76:79], v111 offset:4096\n\t" \
  "v_mfma_f32_16x16x32_bf16 v[4:7], v[48:51], v[96:99], v[4:7]\n\t" \
  "v_mfma_f32_16x16x32_bf16 v[8:11], v[48:51], v[100:103], v[8:11]\n\t" \
  "v_mfma_f32_16x16x32_bf16 v[12:15], v[52:55], v[92:95], v[12:15]\n\t" \
  "v_mfma_f32_16x16x32_bf16 v[16:19], v[52:55], v[96:99], v[16:19]\n\t" \
  "v_mfma_f32_16x16x32_bf16 v[20:23], v[52:55], v[100:103], v[20:23]\n\t" \
  "s_waitcnt lgkmcnt(0)\n\t" \
  "s_barrier\n\t" \
  "v_mfma_f32_16x16x32_bf16 v[0:3], v[24:27], v[56:59], v[0:3]\n\t" \
  "ds_read_b64_tr_b16 v[40:41], v104 offset:16384\n\t" \
  "ds_read_b64_tr_b16 v[42:43], v105 offset:16384\n\t" \
  "v_mfma_f32_16x16x32_bf16 v[4:7], v[24:27], v[60:63], v[4:7]\n\t" \
  "ds_read_b64_tr_b16 v[44:45], v106 offset:16384\n\t" \
  "ds_read_b64_tr_b16 v[46:47], v107 offset:16384\n\t" \
  "v_mfma_f32_16x16x32_bf16 v[8:11], v[24:27], v[64:67], v[8:11]\n\t" \
  "ds_read_b128 v[80:83], v110 offset:6144\n\t" \
  "ds_read_b128 v[84:87], v110 offset:8192\n\t" \
  "v_mfma_f32_16x16x32_bf16 v[12:15], v[28:31], v[56:59], v[12:15]\n\t" \
  "ds_read_b128 v[88:91], v110 offset:10240\n\t" \
  "ds_read_b64_tr_b16 v[48:49], v104 offset:24576\n\t" \
  "v_mfma_f32_16x16x32_bf16 v[16:19], v[28:31], v[60:63], v[16:19]\n\t" \
  "ds_read_b64_tr_b16 v[50:51], v105 offset:24576\n\t" \
  "ds_read_b64_tr_b16 v[52:53], v106 offset:24576\n\t" \
  "v_mfma_f32_16x16x32_bf16 v[20:23], v[28:31], v[64:67], v[20:23]\n\t" \
  "ds_read_b64_tr_b16 v[54:55], v107 offset:24576\n\t" \
  "ds_read_b128 v[92:95], v111 offset:6144\n\t" \
  "v_mfma_f32_16x16x32_bf16 v[0:3], v[32:35], v[68:71], v[0:3]\n\t" \
  "ds_read_b128 v[96:99], v111 offset:8192\n\t" \
  "ds_read_b128 v[100:103], v111 offset:10240\n\t" \
  "v_mfma_f32_16x16x32_bf16 v[4:7], v[32:35], v[72:75], v[4:7]\n\t" \
  "v_mfma_f32_16x16x32_bf16 v[8:11], v[32:35], v[76:79], v[8:11]\n\t" \
  "v_mfma_f32_16x16x32_bf16 v[12:15], v[36:39], v[68:71], v[12:15]\n\t" \
  "v_mfma_f32_16x16x32_bf16 v[16:19], v[36:39], v[72:75], v[16:19]\n\t" \
  "v_mfma_f32_16x16x32_bf16 v[20:23], v[36:39], v[76:79], v[20:23]\n\t" \
  "s_waitcnt lgkmcnt(0)\n\t" \
  "s_barrier\n\t" \
  "v_mfma_f32_16x16x32_bf16 v[0:3], v[40:43], v[80:83], v[0:3]\n\t" \
  "ds_read_b64_tr_b16 v[24:25], v104 offset:32768\n\t" \
  "ds_read_b64_tr_b16 v[26:27], v105 offset:32768\n\t" \
  "v_mfma_f32_16x16x32_bf16 v[4:7], v[40:43], v[84:87], v[4:7]\n\t" \
  "ds_read_b64_tr_b16 v[28:29], v106 offset:32768\n\t" \
  "ds_read_b64_tr_b16 v[30:31], v107 offset:32768\n\t" \
  "v_mfma_f32_16x16x32_bf16 v[8:11], v[40:43], v[88:91], v[8:11]\n\t" \
  "ds_read_b128 v[56:59], v110 offset:12288\n\t" \
  "ds_read_b128 v[60:63], v110 offset:14336\n\t" \
  "v_mfma_f32_16x16x32_bf16 v[12:15], v[44:47], v[80:83], v[12:15]\n\t" \
  "ds_read_b128 v[64:67], v110 offset:16384\n\t" \
  "ds_read_b64_tr_b16 v[32:33], v104 offset:40960\n\t" \
  "v_mfma_f32_16x16x32_bf16 v[16:19], v[44:47], v[84:87], v[16:19]\n\t" \
  "ds_read_b64_tr_b16 v[34:35], v105 offset:40960\n\t" \
  "ds_read_b64_tr_b16 v[36:37], v106 offset:40960\n\t" \
  "v_mfma_f32_16x16x32_bf16 v[20:23], v[44:47], v[88:91], v[20:23]\n\t" \
  "ds_read_b64_tr_b16 v[38:39], v107 offset:40960\n\t" \
  "ds_read_b128 v[68:71], v111 offset:12288\n\t" \
  "v_mfma_f32_16x16x32_bf16 v[0:3], v[48:51], v[92:95], v[0:3]\n\t" \
  "ds_read_b128 v[72:75], v111 offset:14336\n\t" \
  "ds_read_b128 v[76:79], v111 offset:16384\n\t" \
  "v_mfma_f32_16x16x32_bf16 v[4:7], v[48:51], v[96:99], v[4:7]\n\t" \
  "v_mfma_f32_16x16x32_bf16 v[8:11], v[48:51], v[100:103], v[8:11]\n\t" \
  "v_mfma_f32_16x16x32_bf16 v[12:15], v[52:55], v[92:95], v[12:15]\n\t" \
  "v_mfma_f32_16x16x32_bf16 v[16:19], v[52:55], v[96:99], v[16:19]\n\t" \
  "v_mfma_f32_16x16x32_bf16 v[20:23], v[52:55], v[100:103], v[20:23]\n\t" \
  "s_waitcnt lgkmcnt(0)\n\t" \
  "s_barrier\n\t" \
  "v_mfma_f32_16x16x32_bf16 v[0:3], v[24:27], v[56:59], v[0:3]\n\t" \
  "ds_read_b64_tr_b16 v[40:41], v104 offset:49152\n\t" \
  "ds_read_b64_tr_b16 v[42:43], v105 offset:49152\n\t" \
  "v_mfma_f32_16x16x32_bf16 v[4:7], v[24:27], v[60:63], v[4:7]\n\t" \
  "ds_read_b64_tr_b16 v[44:45], v106 offset:49152\n\t" \
  "ds_read_b64_tr_b16 v[46:47], v107 offset:49152\n\t" \
  "v_mfma_f32_16x16x32_bf16 v[8:11], v[24:27], v[64:67], v[8:11]\n\t" \
  "ds_read_b128 v[80:83], v110 offset:18432\n\t" \
  "ds_read_b128 v[84:87], v110 offset:20480\n\t" \
  "v_mfma_f32_16x16x32_bf16 v[12:15], v[28:31], v[56:59], v[12:15]\n\t" \
  "ds_read_b128 v[88:91], v110 offset:22528\n\t" \
  "ds_read_b64_tr_b16 v[48:49], v104 offset:57344\n\t" \
  "v_mfma_f32_16x16x32_bf16 v[16:19], v[28:31], v[60:63], v[16:19]\n\t" \
  "ds_read_b64_tr_b16 v[50:51], v105 offset:57344\n\t" \
  "ds_read_b64_tr_b16 v[52:53], v106 offset:57344\n\t" \
  "v_mfma_f32_16x16x32_bf16 v[20:23], v[28:31], v[64:67], v[20:23]\n\t" \
  "ds_read_b64_tr_b16 v[54:55], v107 offset:57344\n\t" \
  "ds_read_b128 v[92:95], v111 offset:18432\n\t" \
  "v_mfma_f32_16x16x32_bf16 v[0:3], v[32:35], v[68:71], v[0:3]\n\t" \
  "ds_read_b128 v[96:99], v111 offset:20480\n\t" \
  "ds_read_b128 v[100:103], v111 offset:22528\n\t" \
  "v_mfma_f32_16x16x32_bf16 v[4:7], v[32:35], v[72:75], v[4:7]\n\t" \
  "v_mfma_f32_16x16x32_bf16 v[8:11], v[32:35], v[76:79], v[8:11]\n\t" \
  "v_mfma_f32_16x16x32_bf16 v[12:15], v[36:39], v[68:71], v[12:15]\n\t" \
  "v_mfma_f32_16x16x32_bf16 v[16:19], v[36:39], v[72:75], v[16:19]\n\t" \
  "v_mfma_f32_16x16x32_bf16 v[20:23], v[36:39], v[76:79], v[20:23]\n\t" \
  "s_waitcnt lgkmcnt(0)\n\t" \
  "s_barrier\n\t" \
  "v_mfma_f32_16x16x32_bf16 v[0:3], v[40:43], v[80:83], v[0:3]\n\t" \
  "ds_read_b64_tr_b16 v[24:25], v104 offset:0\n\t" \
  "ds_read_b64_tr_b16 v[26:27], v105 offset:0\n\t" \
  "v_mfma_f32_16x16x32_bf16 v[4:7], v[40:43], v[84:87], v[4:7]\n\t" \
  "ds_read_b64_tr_b16 v[28:29], v106 offset:0\n\t" \
  "ds_read_b64_tr_b16 v[30:31], v107 offset:0\n\t" \
  "v_mfma_f32_16x16x32_bf16 v[8:11], v[40:43], v[88:91], v[8:11]\n\t" \
  "ds_read_b128 v[56:59], v110 offset:24576\n\t" \
  "ds_read_b128 v[60:63], v110 offset:26624\n\t" \
  "v_mfma_f32_16x16x32_bf16 v[12:15], v[44:47], v[80:83], v[12:15]\n\t" \
  "ds_read_b128 v[64:67], v110 offset:28672\n\t" \
  "ds_read_b64_tr_b16 v[32:33], v104 offset:8192\n\t" \
  "v_mfma_f32_16x16x32_bf16 v[16:19], v[44:47], v[84:87], v[16:19]\n\t" \
  "ds_read_b64_tr_b16 v[34:35], v105 offset:8192\n\t" \
  "ds_read_b64_tr_b16 v[36:37], v106 offset:8192\n\t" \
  "v_mfma_f32_16x16x32_bf16 v[20:23], v[44:47], v[88:91], v[20:23]\n\t" \
  "ds_read_b64_tr_b16 v[38:39], v107 offset:8192\n\t" \
  "ds_read_b128 v[68:71], v111 offset:24576\n\t" \
  "v_mfma_f32_16x16x32_bf16 v[0:3], v[48:51], v[92:95], v[0:3]\n\t" \
  "ds_read_b128 v[72:75], v111 offset:26624\n\t" \
  "ds_read_b128 v[76:79], v111 offset:28672\n\t" \
  "v_mfma_f32_16x16x32_bf16 v[4:7], v[48:51], v[96:99], v[4:7]\n\t" \
  "v_mfma_f32_16x16x32_bf16 v[8:11], v[48:51], v[100:103], v[8:11]\n\t" \
  "v_mfma_f32_16x16x32_bf16 v[12:15], v[52:55], v[92:95], v[12:15]\n\t" \
  "v_mfma_f32_16x16x32_bf16 v[16:19], v[52:55], v[96:99], v[16:19]\n\t" \
  "v_mfma_f32_16x16x32_bf16 v[20:23], v[52:55], v[100:103], v[20:23]\n\t" \
  "s_waitcnt lgkmcnt(0)\n\t" \
  "s_barrier\n\t" \
  "v_mfma_f32_16x16x32_bf16 v[0:3], v[24:27], v[56:59], v[0:3]\n\t" \
  "ds_read_b64_tr_b16 v[40:41], v104 offset:16384\n\t" \
  "ds_read_b64_tr_b16 v[42:43], v105 offset:16384\n\t" \
  "v_mfma_f32_16x16x32_bf16 v[4:7], v[24:27], v[60:63], v[4:7]\n\t" \
  "ds_read_b64_tr_b16 v[44:45], v106 offset:16384\n\t" \
  "ds_read_b64_tr_b16 v[46:47], v107 offset:16384\n\t" \
  "v_mfma_f32_16x16x32_bf16 v[8:11], v[24:27], v[64:67], v[8:11]\n\t" \
  "ds_read_b128 v[80:83], v110 offset:30720\n\t" \
  "ds_read_b128 v[84:87], v110 offset:32768\n\t" \
  "v_mfma_f32_16x16x32_bf16 v[12:15], v[28:31], v[56:59], v[12:15]\n\t" \
  "ds_read_b128 v[88:91], v110 offset:34816\n\t" \
  "ds_read_b64_tr_b16 v[48:49], v104 offset:24576\n\t" \
  "v_mfma_f32_16x16x32_bf16 v[16:19], v[28:31], v[60:63], v[16:19]\n\t" \
  "ds_read_b64_tr_b16 v[50:51], v105 offset:24576\n\t" \
  "ds_read_b64_tr_b16 v[52:53], v106 offset:24576\n\t" \
  "v_mfma_f32_16x16x32_bf16 v[20:23], v[28:31], v[64:67], v[20:23]\n\t" \
  "ds_read_b64_tr_b16 v[54:55], v107 offset:24576\n\t" \
  "ds_read_b128 v[92:95], v111 offset:30720\n\t" \
  "v_mfma_f32_16x16x32_bf16 v[0:3], v[32:35], v[68:71], v[0:3]\n\t" \
  "ds_read_b128 v[96:99], v111 offset:32768\n\t" \
  "ds_read_b128 v[100:103], v111 offset:34816\n\t" \
  "v_mfma_f32_16x16x32_bf16 v[4:7], v[32:35], v[72:75], v[4:7]\n\t" \
  "v_mfma_f32_16x16x32_bf16 v[8:11], v[32:35], v[76:79], v[8:11]\n\t" \
  "v_mfma_f32_16x16x32_bf16 v[12:15], v[36:39], v[68:71], v[12:15]\n\t" \
  "v_mfma_f32_16x16x32_bf16 v[16:19], v[36:39], v[72:75], v[16:19]\n\t" \
  "v_mfma_f32_16x16x32_bf16 v[20:23], v[36:39], v[76:79], v[20:23]\n\t" \
  "s_waitcnt lgkmcnt(0)\n\t" \
  "s_barrier\n\t" \
  "v_mfma_f32_16x16x32_bf16 v[0:3], v[40:43], v[80:83], v[0:3]\n\t" \
  "ds_read_b64_tr_b16 v[24:25], v104 offset:32768\n\t" \
  "ds_read_b64_tr_b16 v[26:27], v105 offset:32768\n\t" \
  "v_mfma_f32_16x16x32_bf16 v[4:7], v[40:43], v[84:87], v[4:7]\n\t" \
  "ds_read_b64_tr_b16 v[28:29], v106 offset:32768\n\t" \
  "ds_read_b64_tr_b16 v[30:31], v107 offset:32768\n\t" \
  "v_mfma_f32_16x16x32_bf16 v[8:11], v[40:43], v[88:91], v[8:11]\n\t" \
  "ds_read_b128 v[56:59], v110 offset:36864\n\t" \
  "ds_read_b128 v[60:63], v110 offset:38912\n\t" \
  "v_mfma_f32_16x16x32_bf16 v[12:15], v[44:47], v[80:83], v[12:15]\n\t" \
  "ds_read_b128 v[64:67], v110 offset:40960\n\t" \
  "ds_read_b64_tr_b16 v[32:33], v104 offset:40960\n\t" \
  "v_mfma_f32_16x16x32_bf16 v[16:19], v[44:47], v[84:87], v[16:19]\n\t" \
  "ds_read_b64_tr_b16 v[34:35], v105 offset:40960\n\t" \
  "ds_read_b64_tr_b16 v[36:37], v106 offset:40960\n\t" \
  "v_mfma_f32_16x16x32_bf16 v[20:23], v[44:47], v[88:91], v[20:23]\n\t" \
  "ds_read_b64_tr_b16 v[38:39], v107 offset:40960\n\t" \
  "ds_read_b128 v[68:71], v111 offset:36864\n\t" \
  "v_mfma_f32_16x16x32_bf16 v[0:3], v[48:51], v[92:95], v[0:3]\n\t" \
  "ds_read_b128 v[72:75], v111 offset:38912\n\t" \
  "ds_read_b128 v[76:79], v111 offset:40960\n\t" \
  "v_mfma_f32_16x16x32_bf16 v[4:7], v[48:51], v[96:99], v[4:7]\n\t" \
  "v_mfma_f32_16x16x32_bf16 v[8:11], v[48:51], v[100:103], v[8:11]\n\t" \
  "v_mfma_f32_16x16x32_bf16 v[12:15], v[52:55], v[92:95], v[12:15]\n\t" \
  "v_mfma_f32_16x16x32_bf16 v[16:19], v[52:55], v[96:99], v[16:19]\n\t" \
  "v_mfma_f32_16x16x32_bf16 v[20:23], v[52:55], v[100:103], v[20:23]\n\t" \
  "s_waitcnt lgkmcnt(0)\n\t" \
  "s_barrier\n\t" \
  "v_mfma_f32_16x16x32_bf16 v[0:3], v[24:27], v[56:59], v[0:3]\n\t" \
  "ds_read_b64_tr_b16 v[40:41], v104 offset:49152\n\t" \
  "ds_read_b64_tr_b16 v[42:43], v105 offset:49152\n\t" \
  "v_mfma_f32_16x16x32_bf16 v[4:7], v[24:27], v[60:63], v[4:7]\n\t" \
  "ds_read_b64_tr_b16 v[44:45], v106 offset:49152\n\t" \
  "ds_read_b64_tr_b16 v[46:47], v107 offset:49152\n\t" \
  "v_mfma_f32_16x16x32_bf16 v[8:11], v[24:27], v[64:67], v[8:11]\n\t" \
  "ds_read_b128 v[80:83], v110 offset:43008\n\t" \
  "ds_read_b128 v[84:87], v110 offset:45056\n\t" \
  "v_mfma_f32_16x16x32_bf16 v[12:15], v[28:31], v[56:59], v[12:15]\n\t" \
  "ds_read_b128 v[88:91], v110 offset:47104\n\t" \
  "ds_read_b64_tr_b16 v[48:49], v104 offset:57344\n\t" \
  "v_mfma_f32_16x16x32_bf16 v[16:19], v[28:31], v[60:63], v[16:19]\n\t" \
  "ds_read_b64_tr_b16 v[50:51], v105 offset:57344\n\t" \
  "ds_read_b64_tr_b16 v[52:53], v106 offset:57344\n\t" \
  "v_mfma_f32_16x16x32_bf16 v[20:23], v[28:31], v[64:67], v[20:23]\n\t" \
  "ds_read_b64_tr_b16 v[54:55], v107 offset:57344\n\t" \
  "ds_read_b128 v[92:95], v111 offset:43008\n\t" \
  "v_mfma_f32_16x16x32_bf16 v[0:3], v[32:35], v[68:71], v[0:3]\n\t" \
  "ds_read_b128 v[96:99], v111 offset:45056\n\t" \
  "ds_read_b128 v[100:103], v111 offset:47104\n\t" \
  "v_mfma_f32_16x16x32_bf16 v[4:7], v[32:35], v[72:75], v[4:7]\n\t" \
  "v_mfma_f32_16x16x32_bf16 v[8:11], v[32:35], v[76:79], v[8:11]\n\t" \
  "v_mfma_f32_16x16x32_bf16 v[12:15], v[36:39], v[68:71], v[12:15]\n\t" \
  "v_mfma_f32_16x16x32_bf16 v[16:19], v[36:39], v[72:75], v[16:19]\n\t" \
  "v_mfma_f32_16x16x32_bf16 v[20:23], v[36:39], v[76:79], v[20:23]\n\t" \
  "s_waitcnt lgkmcnt(9)\n\t" \
  "v_mfma_f32_16x16x32_bf16 v[0:3], v[40:43], v[80:83], v[0:3]\n\t" \
  "s_waitcnt lgkmcnt(8)\n\t" \
  "v_mfma_f32_16x16x32_bf16 v[4:7], v[40:43], v[84:87], v[4:7]\n\t" \
  "s_waitcnt lgkmcnt(7)\n\t" \
  "v_mfma_f32_16x16x32_bf16 v[8:11], v[40:43], v[88:91], v[8:11]\n\t" \
  "v_mfma_f32_16x16x32_bf16 v[12:15], v[44:47], v[80:83], v[12:15]\n\t" \
  "v_mfma_f32_16x16x32_bf16 v[16:19], v[44:47], v[84:87], v[16:19]\n\t" \
  "v_mfma_f32_16x16x32_bf16 v[20:23], v[44:47], v[88:91], v[20:23]\n\t" \
  "s_waitcnt lgkmcnt(2)\n\t" \
  "v_mfma_f32_16x16x32_bf16 v[0:3], v[48:51], v[92:95], v[0:3]\n\t" \
  "s_waitcnt lgkmcnt(1)\n\t" \
  "v_mfma_f32_16x16x32_bf16 v[4:7], v[48:51], v[96:99], v[4:7]\n\t" \
  "s_waitcnt lgkmcnt(0)\n\t" \
  "v_mfma_f32_16x16x32_bf16 v[8:11], v[48:51], v[100:103], v[8:11]\n\t" \
  "v_mfma_f32_16x16x32_bf16 v[12:15], v[52:55], v[92:95], v[12:15]\n\t" \
  "v_mfma_f32_16x16x32_bf16 v[16:19], v[52:55], v[96:99], v[16:19]\n\t" \
  "v_mfma_f32_16x16x32_bf16 v[20:23], v[52:55], v[100:103], v[20:23]\n\t" \
  "s_nop 7\n\t" \
  "s_nop 1\n\t"
#define GEMM_QT_ASM \
  "s_barrier\n\t" \
  "ds_read_b64_tr_b16 v[24:25], v104 offset:0\n\t" \
  "ds_read_b64_tr_b16 v[26:27], v105 offset:0\n\t" \
  "ds_read_b64_tr_b16 v[28:29], v106 offset:0\n\t" \
  "ds_read_b64_tr_b16 v[30:31], v107 offset:0\n\t" \
  "ds_read_b128 v[56:59], v108 offset:0\n\t" \
  "ds_read_b128 v[60:63], v108 offset:2048\n\t" \
  "ds_read_b128 v[64:67], v108 offset:4096\n\t" \
  "ds_read_b64_tr_b16 v[32:33], v104 offset:8192\n\t" \
  "ds_read_b64_tr_b16 v[34:35], v105 offset:8192\n\t" \
  "ds_read_b64_tr_b16 v[36:37], v106 offset:8192\n\t" \
  "ds_read_b64_tr_b16 v[38:39], v107 offset:8192\n\t" \
  "ds_read_b128 v[68:71], v109 offset:0\n\t" \
  "ds_read_b128 v[72:75], v109 offset:2048\n\t" \
  "ds_read_b128 v[76:79], v109 offset:4096\n\t" \
  "s_waitcnt lgkmcnt(0)\n\t" \
  "s_barrier\n\t" \
  "v_mfma_f32_16x16x32_bf16 v[0:3], v[24:27], v[56:59], 0\n\t" \
  "ds_read_b64_tr_b16 v[40:41], v104 offset:16384\n\t" \
  "ds_read_b64_tr_b16 v[42:43], v105 offset:16384\n\t" \
  "v_mfma_f32_16x16x32_bf16 v[4:7], v[24:27], v[60:63], 0\n\t" \
  "ds_read_b64_tr_b16 v[44:45], v106 offset:16384\n\t" \
  "ds_read_b64_tr_b16 v[46:47], v107 offset:16384\n\t" \
  "v_mfma_f32_16x16x32_bf16 v[8:11], v[24:27], v[64:67], 0\n\t" \
  "ds_read_b128 v[80:83], v108 offset:6144\n\t" \
  "ds_read_b128 v[84:87], v108 offset:8192\n\t" \
  "v_mfma_f32_16x16x32_bf16 v[12:15], v[28:31], v[56:59], 0\n\t" \
  "ds_read_b128 v[88:91], v108 offset:10240\n\t" \
  "ds_read_b64_tr_b16 v[48:49], v104 offset:24576\n\t" \
  "v_mfma_f32_16x16x32_bf16 v[16:19], v[28:31], v[60:63], 0\n\t" \
  "ds_read_b64_tr_b16 v[50:51], v105 offset:24576\n\t" \
  "ds_read_b64_tr_b16 v[52:53], v106 offset:24576\n\t" \
  "v_mfma_f32_16x16x32_bf16 v[20:23], v[28:31], v[64:67], 0\n\t" \
  "ds_read_b64_tr_b16 v[54:55], v107 offset:24576\n\t" \
  "ds_read_b128 v[92:95], v109 offset:6144\n\t" \
  "v_mfma_f32_16x16x32_bf16 v[0:3], v[32:35], v[68:71], v[0:3]\n\t" \
  "ds_read_b128 v[96:99], v109 offset:8192\n\t" \
  "ds_read_b128 v[100:103], v109 offset:10240\n\t" \
  "v_mfma_f32_16x16x32_bf16 v[4:7], v[32:35], v[72:75], v[4:7]\n\t" \
  "v_mfma_f32_16x16x32_bf16 v[8:11], v[32:35], v[76:79], v[8:11]\n\t" \
  "v_mfma_f32_16x16x32_bf16 v[12:15], v[36:39], v[68:71], v[12:15]\n\t" \
  "v_mfma_f32_16x16x32_bf16 v[16:19], v[36:39], v[72:75], v[16:19]\n\t" \
  "v_mfma_f32_16x16x32_bf16 v[20:23], v[36:39], v[76:79], v[20:23]\n\t" \
  "s_waitcnt lgkmcnt(0)\n\t" \
  "s_barrier\n\t" \
  "v_mfma_f32_16x16x32_bf16 v[0:3], v[40:43], v[80:83], v[0:3]\n\t" \
  "ds_read_b64_tr_b16 v[24:25], v104 offset:32768\n\t" \
  "ds_read_b64_tr_b16 v[26:27], v105 offset:32768\n\t" \
  "v_mfma_f32_16x16x32_bf16 v[4:7], v[40:43], v[84:87], v[4:7]\n\t" \
  "ds_read_b64_tr_b16 v[28:29], v106 offset:32768\n\t" \
  "ds_read_b64_tr_b16 v[30:31], v107 offset:32768\n\t" \
  "v_mfma_f32_16x16x32_bf16 v[8:11], v[40:43], v[88:91], v[8:11]\n\t" \
  "ds_read_b128 v[56:59], v108 offset:12288\n\t" \
  "ds_read_b128 v[60:63], v108 offset:14336\n\t" \
  "v_mfma_f32_16x16x32_bf16 v[12:15], v[44:47], v[80:83], v[12:15]\n\t" \
  "ds_read_b128 v[64:67], v108 offset:16384\n\t" \
  "ds_read_b64_tr_b16 v[32:33], v104 offset:40960\n\t" \
  "v_mfma_f32_16x16x32_bf16 v[16:19], v[44:47], v[84:87], v[16:19]\n\t" \
  "ds_read_b64_tr_b16 v[34:35], v105 offset:40960\n\t" \
  "ds_read_b64_tr_b16 v[36:37], v106 offset:40960\n\t" \
  "v_mfma_f32_16x16x32_bf16 v[20:23], v[44:47], v[88:91], v[20:23]\n\t" \
  "ds_read_b64_tr_b16 v[38:39], v107 offset:40960\n\t" \
  "ds_read_b128 v[68:71], v109 offset:12288\n\t" \
  "v_mfma_f32_16x16x32_bf16 v[0:3], v[48:51], v[92:95], v[0:3]\n\t" \
  "ds_read_b128 v[72:75], v109 offset:14336\n\t" \
  "ds_read_b128 v[76:79], v109 offset:16384\n\t" \
  "v_mfma_f32_16x16x32_bf16 v[4:7], v[48:51], v[96:99], v[4:7]\n\t" \
  "v_mfma_f32_16x16x32_bf16 v[8:11], v[48:51], v[100:103], v[8:11]\n\t" \
  "v_mfma_f32_16x16x32_bf16 v[12:15], v[52:55], v[92:95], v[12:15]\n\t" \
  "v_mfma_f32_16x16x32_bf16 v[16:19], v[52:55], v[96:99], v[16:19]\n\t" \
  "v_mfma_f32_16x16x32_bf16 v[20:23], v[52:55], v[100:103], v[20:23]\n\t" \
  "s_waitcnt lgkmcnt(0)\n\t" \
  "s_barrier\n\t" \
  "v_mfma_f32_16x16x32_bf16 v[0:3], v[24:27], v[56:59], v[0:3]\n\t" \
  "ds_read_b64_tr_b16 v[40:41], v104 offset:49152\n\t" \
  "ds_read_b64_tr_b16 v[42:43], v105 offset:49152\n\t" \
  "v_mfma_f32_16x16x32_bf16 v[4:7], v[24:27], v[60:63], v[4:7]\n\t" \
  "ds_read_b64_tr_b16 v[44:45], v106 offset:49152\n\t" \
  "ds_read_b64_tr_b16 v[46:47], v107 offset:49152\n\t" \
  "v_mfma_f32_16x16x32_bf16 v[8:11], v[24:27], v[64:67], v[8:11]\n\t" \
  "ds_read_b128 v[80:83], v108 offset:18432\n\t" \
  "ds_read_b128 v[84:87], v108 offset:20480\n\t" \
  "v_mfma_f32_16x16x32_bf16 v[12:15], v[28:31], v[56:59], v[12:15]\n\t" \
  "ds_read_b128 v[88:91], v108 offset:22528\n\t" \
  "ds_read_b64_tr_b16 v[48:49], v104 offset:57344\n\t" \
  "v_mfma_f32_16x16x32_bf16 v[16:19], v[28:31], v[60:63], v[16:19]\n\t" \
  "ds_read_b64_tr_b16 v[50:51], v105 offset:57344\n\t" \
  "ds_read_b64_tr_b16 v[52:53], v106 offset:57344\n\t" \
  "v_mfma_f32_16x16x32_bf16 v[20:23], v[28:31], v[64:67], v[20:23]\n\t" \
  "ds_read_b64_tr_b16 v[54:55], v107 offset:57344\n\t" \
  "ds_read_b128 v[92:95], v109 offset:18432\n\t" \
  "v_mfma_f32_16x16x32_bf16 v[0:3], v[32:35], v[68:71], v[0:3]\n\t" \
  "ds_read_b128 v[96:99], v109 offset:20480\n\t" \
  "ds_read_b128 v[100:103], v109 offset:22528\n\t" \
  "v_mfma_f32_16x16x32_bf16 v[4:7], v[32:35], v[72:75], v[4:7]\n\t" \
  "v_mfma_f32_16x16x32_bf16 v[8:11], v[32:35], v[76:79], v[8:11]\n\t" \
  "v_mfma_f32_16x16x32_bf16 v[12:15], v[36:39], v[68:71], v[12:15]\n\t" \
  "v_mfma_f32_16x16x32_bf16 v[16:19], v[36:39], v[72:75], v[16:19]\n\t" \
  "v_mfma_f32_16x16x32_bf16 v[20:23], v[36:39], v[76:79], v[20:23]\n\t" \
  "s_waitcnt lgkmcnt(0)\n\t" \
  "s_barrier\n\t" \
  "v_mfma_f32_16x16x32_bf16 v[0:3], v[40:43], v[80:83], v[0:3]\n\t" \
  "ds_read_b64_tr_b16 v[24:25], v104 offset:0\n\t" \
  "ds_read_b64_tr_b16 v[26:27], v105 offset:0\n\t" \
  "v_mfma_f32_16x16x32_bf16 v[4:7], v[40:43], v[84:87], v[4:7]\n\t" \
  "ds_read_b64_tr_b16 v[28:29], v106 offset:0\n\t" \
  "ds_read_b64_tr_b16 v[30:31], v107 offset:0\n\t" \
  "v_mfma_f32_16x16x32_bf16 v[8:11], v[40:43], v[88:91], v[8:11]\n\t" \
  "ds_read_b128 v[56:59], v108 offset:24576\n\t" \
  "ds_read_b128 v[60:63], v108 offset:26624\n\t" \
  "v_mfma_f32_16x16x32_bf16 v[12:15], v[44:47], v[80:83], v[12:15]\n\t" \
  "ds_read_b128 v[64:67], v108 offset:28672\n\t" \
  "ds_read_b64_tr_b16 v[32:33], v104 offset:8192\n\t" \
  "v_mfma_f32_16x16x32_bf16 v[16:19], v[44:47], v[84:87], v[16:19]\n\t" \
  "ds_read_b64_tr_b16 v[34:35], v105 offset:8192\n\t" \
  "ds_read_b64_tr_b16 v[36:37], v106 offset:8192\n\t" \
  "v_mfma_f32_16x16x32_bf16 v[20:23], v[44:47], v[88:91], v[20:23]\n\t" \
  "ds_read_b64_tr_b16 v[38:39], v107 offset:8192\n\t" \
  "ds_read_b128 v[68:71], v109 offset:24576\n\t" \
  "v_mfma_f32_16x16x32_bf16 v[0:3], v[48:51], v[92:95], v[0:3]\n\t" \
  "ds_read_b128 v[72:75], v109 offset:26624\n\t" \
  "ds_read_b128 v[76:79], v109 offset:28672\n\t" \
  "v_mfma_f32_16x16x32_bf16 v[4:7], v[48:51], v[96:99], v[4:7]\n\t" \
  "v_mfma_f32_16x16x32_bf16 v[8:11], v[48:51], v[100:103], v[8:11]\n\t" \
  "v_mfma_f32_16x16x32_bf16 v[12:15], v[52:55], v[92:95], v[12:15]\n\t" \
  "v_mfma_f32_16x16x32_bf16 v[16:19], v[52:55], v[96:99], v[16:19]\n\t" \
  "v_mfma_f32_16x16x32_bf16 v[20:23], v[52:55], v[100:103], v[20:23]\n\t" \
  "s_waitcnt lgkmcnt(0)\n\t" \
  "s_barrier\n\t" \
  "v_mfma_f32_16x16x32_bf16 v[0:3], v[24:27], v[56:59], v[0:3]\n\t" \
  "ds_read_b64_tr_b16 v[40:41], v104 offset:16384\n\t" \
  "ds_read_b64_tr_b16 v[42:43], v105 offset:16384\n\t" \
  "v_mfma_f32_16x16x32_bf16 v[4:7], v[24:27], v[60:63], v[4:7]\n\t" \
  "ds_read_b64_tr_b16 v[44:45], v106 offset:16384\n\t" \
  "ds_read_b64_tr_b16 v[46:47], v107 offset:16384\n\t" \
  "v_mfma_f32_16x16x32_bf16 v[8:11], v[24:27], v[64:67], v[8:11]\n\t" \
  "ds_read_b128 v[80:83], v108 offset:30720\n\t" \
  "ds_read_b128 v[84:87], v108 offset:32768\n\t" \
  "v_mfma_f32_16x16x32_bf16 v[12:15], v[28:31], v[56:59], v[12:15]\n\t" \
  "ds_read_b128 v[88:91], v108 offset:34816\n\t" \
  "ds_read_b64_tr_b16 v[48:49], v104 offset:24576\n\t" \
  "v_mfma_f32_16x16x32_bf16 v[16:19], v[28:31], v[60:63], v[16:19]\n\t" \
  "ds_read_b64_tr_b16 v[50:51], v105 offset:24576\n\t" \
  "ds_read_b64_tr_b16 v[52:53], v106 offset:24576\n\t" \
  "v_mfma_f32_16x16x32_bf16 v[20:23], v[28:31], v[64:67], v[20:23]\n\t" \
  "ds_read_b64_tr_b16 v[54:55], v107 offset:24576\n\t" \
  "ds_read_b128 v[92:95], v109 offset:30720\n\t" \
  "v_mfma_f32_16x16x32_bf16 v[0:3], v[32:35], v[68:71], v[0:3]\n\t" \
  "ds_read_b128 v[96:99], v109 offset:32768\n\t" \
  "ds_read_b128 v[100:103], v109 offset:34816\n\t" \
  "v_mfma_f32_16x16x32_bf16 v[4:7], v[32:35], v[72:75], v[4:7]\n\t" \
  "v_mfma_f32_16x16x32_bf16 v[8:11], v[32:35], v[76:79], v[8:11]\n\t" \
  "v_mfma_f32_16x16x32_bf16 v[12:15], v[36:39], v[68:71], v[12:15]\n\t" \
  "v_mfma_f32_16x16x32_bf16 v[16:19], v[36:39], v[72:75], v[16:19]\n\t" \
  "v_mfma_f32_16x16x32_bf16 v[20:23], v[36:39], v[76:79], v[20:23]\n\t" \
  "s_waitcnt lgkmcnt(0)\n\t" \
  "s_barrier\n\t" \
  "v_mfma_f32_16x16x32_bf16 v[0:3], v[40:43], v[80:83], v[0:3]\n\t" \
  "ds_read_b64_tr_b16 v[24:25], v104 offset:32768\n\t" \
  "ds_read_b64_tr_b16 v[26:27], v105 offset:32768\n\t" \
  "v_mfma_f32_16x16x32_bf16 v[4:7], v[40:43], v[84:87], v[4:7]\n\t" \
  "ds_read_b64_tr_b16 v[28:29], v106 offset:32768\n\t" \
  "ds_read_b64_tr_b16 v[30:31], v107 offset:32768\n\t" \
  "v_mfma_f32_16x16x32_bf16 v[8:11], v[40:43], v[88:91], v[8:11]\n\t" \
  "ds_read_b128 v[56:59], v108 offset:36864\n\t" \
  "ds_read_b128 v[60:63], v108 offset:38912\n\t" \
  "v_mfma_f32_16x16x32_bf16 v[12:15], v[44:47], v[80:83], v[12:15]\n\t" \
  "ds_read_b128 v[64:67], v108 offset:40960\n\t" \
  "ds_read_b64_tr_b16 v[32:33], v104 offset:40960\n\t" \
  "v_mfma_f32_16x16x32_bf16 v[16:19], v[44:47], v[84:87], v[16:19]\n\t" \
  "ds_read_b64_tr_b16 v[34:35], v105 offset:40960\n\t" \
  "ds_read_b64_tr_b16 v[36:37], v106 offset:40960\n\t" \
  "v_mfma_f32_16x16x32_bf16 v[20:23], v[44:47], v[88:91], v[20:23]\n\t" \
  "ds_read_b64_tr_b16 v[38:39], v107 offset:40960\n\t" \
  "ds_read_b128 v[68:71], v109 offset:36864\n\t" \
  "v_mfma_f32_16x16x32_bf16 v[0:3], v[48:51], v[92:95], v[0:3]\n\t" \
  "ds_read_b128 v[72:75], v109 offset:38912\n\t" \
  "ds_read_b128 v[76:79], v109 offset:40960\n\t" \
  "v_mfma_f32_16x16x32_bf16 v[4:7], v[48:51], v[96:99], v[4:7]\n\t" \
  "v_mfma_f32_16x16x32_bf16 v[8:11], v[48:51], v[100:103], v[8:11]\n\t" \
  "v_mfma_f32_16x16x32_bf16 v[12:15], v[52:55], v[92:95], v[12:15]\n\t" \
  "v_mfma_f32_16x16x32_bf16 v[16:19], v[52:55], v[96:99], v[16:19]\n\t" \
  "v_mfma_f32_16x16x32_bf16 v[20:23], v[52:55], v[100:103], v[20:23]\n\t" \
  "s_waitcnt lgkmcnt(0)\n\t" \
  "s_barrier\n\t" \
  "v_mfma_f32_16x16x32_bf16 v[0:3], v[24:27], v[56:59], v[0:3]\n\t" \
  "ds_read_b64_tr_b16 v[40:41], v104 offset:49152\n\t" \
  "ds_read_b64_tr_b16 v[42:43], v105 offset:49152\n\t" \
  "v_mfma_f32_16x16x32_bf16 v[4:7], v[24:27], v[60:63], v[4:7]\n\t" \
  "ds_read_b64_tr_b16 v[44:45], v106 offset:49152\n\t" \
  "ds_read_b64_tr_b16 v[46:47], v107 offset:49152\n\t" \
  "v_mfma_f32_16x16x32_bf16 v[8:11], v[24:27], v[64:67], v[8:11]\n\t" \
  "ds_read_b128 v[80:83], v108 offset:43008\n\t" \
  "ds_read_b128 v[84:87], v108 offset:45056\n\t" \
  "v_mfma_f32_16x16x32_bf16 v[12:15], v[28:31], v[56:59], v[12:15]\n\t" \
  "ds_read_b128 v[88:91], v108 offset:47104\n\t" \
  "ds_read_b64_tr_b16 v[48:49], v104 offset:57344\n\t" \
  "v_mfma_f32_16x16x32_bf16 v[16:19], v[28:31], v[60:63], v[16:19]\n\t" \
  "ds_read_b64_tr_b16 v[50:51], v105 offset:57344\n\t" \
  "ds_read_b64_tr_b16 v[52:53], v106 offset:57344\n\t" \
  "v_mfma_f32_16x16x32_bf16 v[20:23], v[28:31], v[64:67], v[20:23]\n\t" \
  "ds_read_b64_tr_b16 v[54:55], v107 offset:57344\n\t" \
  "ds_read_b128 v[92:95], v109 offset:43008\n\t" \
  "v_mfma_f32_16x16x32_bf16 v[0:3], v[32:35], v[68:71], v[0:3]\n\t" \
  "ds_read_b128 v[96:99], v109 offset:45056\n\t" \
  "ds_read_b128 v[100:103], v109 offset:47104\n\t" \
  "v_mfma_f32_16x16x32_bf16 v[4:7], v[32:35], v[72:75], v[4:7]\n\t" \
  "v_mfma_f32_16x16x32_bf16 v[8:11], v[32:35], v[76:79], v[8:11]\n\t" \
  "v_mfma_f32_16x16x32_bf16 v[12:15], v[36:39], v[68:71], v[12:15]\n\t" \
  "v_mfma_f32_16x16x32_bf16 v[16:19], v[36:39], v[72:75], v[16:19]\n\t" \
  "v_mfma_f32_16x16x32_bf16 v[20:23], v[36:39], v[76:79], v[20:23]\n\t" \
  "s_waitcnt lgkmcnt(0)\n\t" \
  "s_barrier\n\t" \
  "v_mfma_f32_16x16x32_bf16 v[0:3], v[40:43], v[80:83], v[0:3]\n\t" \
  "ds_read_b64_tr_b16 v[24:25], v104 offset:0\n\t" \
  "ds_read_b64_tr_b16 v[26:27], v105 offset:0\n\t" \
  "v_mfma_f32_16x16x32_bf16 v[4:7], v[40:43], v[84:87], v[4:7]\n\t" \
  "ds_read_b64_tr_b16 v[28:29], v106 offset:0\n\t" \
  "ds_read_b64_tr_b16 v[30:31], v107 offset:0\n\t" \
  "v_mfma_f32_16x16x32_bf16 v[8:11], v[40:43], v[88:91], v[8:11]\n\t" \
  "ds_read_b128 v[56:59], v110 offset:0\n\t" \
  "ds_read_b128 v[60:63], v110 offset:2048\n\t" \
  "v_mfma_f32_16x16x32_bf16 v[12:15], v[44:47], v[80:83], v[12:15]\n\t" \
  "ds_read_b128 v[64:67], v110 offset:4096\n\t" \
  "ds_read_b64_tr_b16 v[32:33], v104 offset:8192\n\t" \
  "v_mfma_f32_16x16x32_bf16 v[16:19], v[44:47], v[84:87], v[16:19]\n\t" \
  "ds_read_b64_tr_b16 v[34:35], v105 offset:8192\n\t" \
  "ds_read_b64_tr_b16 v[36:37], v106 offset:8192\n\t" \
  "v_mfma_f32_16x16x32_bf16 v[20:23], v[44:47], v[88:91], v[20:23]\n\t" \
  "ds_read_b64_tr_b16 v[38:39], v107 offset:8192\n\t" \
  "ds_read_b128 v[68:71], v111 offset:0\n\t" \
  "v_mfma_f32_16x16x32_bf16 v[0:3], v[48:51], v[92:95], v[0:3]\n\t" \
  "ds_read_b128 v[72:75], v111 offset:2048\n\t" \
  "ds_read_b128 v[76:79], v111 offset:4096\n\t" \
  "v_mfma_f32_16x16x32_bf16 v[4:7], v[48:51], v[96:99], v[4:7]\n\t" \
  "v_mfma_f32_16x16x32_bf16 v[8:11], v[48:51], v[100:103], v[8:11]\n\t" \
  "v_mfma_f32_16x16x32_bf16 v[12:15], v[52:55], v[92:95], v[12:15]\n\t" \
  "v_mfma_f32_16x16x32_bf16 v[16:19], v[52:55], v[96:99], v[16:19]\n\t" \
  "v_mfma_f32_16x16x32_bf16 v[20:23], v[52:55], v[100:103], v[20:23]\n\t" \
  "s_waitcnt lgkmcnt(0)\n\t" \
  "s_barrier\n\t" \
  "buffer_load_dword v255, v254, %[tks], %[tof] offen\n\t" \
  "v_mfma_f32_16x16x32_bf16 v[0:3], v[24:27], v[56:59], v[0:3]\n\t" \
  "ds_read_b64_tr_b16 v[40:41], v104 offset:16384\n\t" \
  "ds_read_b64_tr_b16 v[42:43], v105 offset:16384\n\t" \
  "v_mfma_f32_16x16x32_bf16 v[4:7], v[24:27], v[60:63], v[4:7]\n\t" \
  "ds_read_b64_tr_b16 v[44:45], v106 offset:16384\n\t" \
  "ds_read_b64_tr_b16 v[46:47], v107 offset:16384\n\t" \
  "v_mfma_f32_16x16x32_bf16 v[8:11], v[24:27], v[64:67], v[8:11]\n\t" \
  "ds_read_b128 v[80:83], v110 offset:6144\n\t" \
  "ds_read_b128 v[84:87], v110 offset:8192\n\t" \
  "v_mfma_f32_16x16x32_bf16 v[12:15], v[28:31], v[56:59], v[12:15]\n\t" \
  "ds_read_b128 v[88:91], v110 offset:10240\n\t" \
  "ds_read_b64_tr_b16 v[48:49], v104 offset:24576\n\t" \
  "v_mfma_f32_16x16x32_bf16 v[16:19], v[28:31], v[60:63], v[16:19]\n\t" \
  "ds_read_b64_tr_b16 v[50:51], v105 offset:24576\n\t" \
  "ds_read_b64_tr_b16 v[52:53], v106 offset:24576\n\t" \
  "v_mfma_f32_16x16x32_bf16 v[20:23], v[28:31], v[64:67], v[20:23]\n\t" \
  "ds_read_b64_tr_b16 v[54:55], v107 offset:24576\n\t" \
  "ds_read_b128 v[92:95], v111 offset:6144\n\t" \
  "v_mfma_f32_16x16x32_bf16 v[0:3], v[32:35], v[68:71], v[0:3]\n\t" \
  "ds_read_b128 v[96:99], v111 offset:8192\n\t" \
  "ds_read_b128 v[100:103], v111 offset:10240\n\t" \
  "v_mfma_f32_16x16x32_bf16 v[4:7], v[32:35], v[72:75], v[4:7]\n\t" \
  "v_mfma_f32_16x16x32_bf16 v[8:11], v[32:35], v[76:79], v[8:11]\n\t" \
  "v_mfma_f32_16x16x32_bf16 v[12:15], v[36:39], v[68:71], v[12:15]\n\t" \
  "v_mfma_f32_16x16x32_bf16 v[16:19], v[36:39], v[72:75], v[16:19]\n\t" \
  "v_mfma_f32_16x16x32_bf16 v[20:23], v[36:39], v[76:79], v[20:23]\n\t" \
  "s_waitcnt lgkmcnt(0)\n\t" \
  "s_barrier\n\t" \
  "v_mfma_f32_16x16x32_bf16 v[0:3], v[40:43], v[80:83], v[0:3]\n\t" \
  "ds_read_b64_tr_b16 v[24:25], v104 offset:32768\n\t" \
  "ds_read_b64_tr_b16 v[26:27], v105 offset:32768\n\t" \
  "v_mfma_f32_16x16x32_bf16 v[4:7], v[40:43], v[84:87], v[4:7]\n\t" \
  "ds_read_b64_tr_b16 v[28:29], v106 offset:32768\n\t" \
  "ds_read_b64_tr_b16 v[30:31], v107 offset:32768\n\t" \
  "v_mfma_f32_16x16x32_bf16 v[8:11], v[40:43], v[88:91], v[8:11]\n\t" \
  "ds_read_b128 v[56:59], v110 offset:12288\n\t" \
  "ds_read_b128 v[60:63], v110 offset:14336\n\t" \
  "v_mfma_f32_16x16x32_bf16 v[12:15], v[44:47], v[80:83], v[12:15]\n\t" \
  "ds_read_b128 v[64:67], v110 offset:16384\n\t" \
  "ds_read_b64_tr_b16 v[32:33], v104 offset:40960\n\t" \
  "v_mfma_f32_16x16x32_bf16 v[16:19], v[44:47], v[84:87], v[16:19]\n\t" \
  "ds_read_b64_tr_b16 v[34:35], v105 offset:40960\n\t" \
  "ds_read_b64_tr_b16 v[36:37], v106 offset:40960\n\t" \
  "v_mfma_f32_16x16x32_bf16 v[20:23], v[44:47], v[88:91], v[20:23]\n\t" \
  "ds_read_b64_tr_b16 v[38:39], v107 offset:40960\n\t" \
  "ds_read_b128 v[68:71], v111 offset:12288\n\t" \
  "v_mfma_f32_16x16x32_bf16 v[0:3], v[48:51], v[92:95], v[0:3]\n\t" \
  "ds_read_b128 v[72:75], v111 offset:14336\n\t" \
  "ds_read_b128 v[76:79], v111 offset:16384\n\t" \
  "v_mfma_f32_16x16x32_bf16 v[4:7], v[48:51], v[96:99], v[4:7]\n\t" \
  "v_mfma_f32_16x16x32_bf16 v[8:11], v[48:51], v[100:103], v[8:11]\n\t" \
  "v_mfma_f32_16x16x32_bf16 v[12:15], v[52:55], v[92:95], v[12:15]\n\t" \
  "v_mfma_f32_16x16x32_bf16 v[16:19], v[52:55], v[96:99], v[16:19]\n\t" \
  "v_mfma_f32_16x16x32_bf16 v[20:23], v[52:55], v[100:103], v[20:23]\n\t" \
  "s_waitcnt lgkmcnt(0)\n\t" \
  "s_barrier\n\t" \
  "v_mfma_f32_16x16x32_bf16 v[0:3], v[24:27], v[56:59], v[0:3]\n\t" \
  "ds_read_b64_tr_b16 v[40:41], v104 offset:49152\n\t" \
  "ds_read_b64_tr_b16 v[42:43], v105 offset:49152\n\t" \
  "v_mfma_f32_16x16x32_bf16 v[4:7], v[24:27], v[60:63], v[4:7]\n\t" \
  "ds_read_b64_tr_b16 v[44:45], v106 offset:49152\n\t" \
  "ds_read_b64_tr_b16 v[46:47], v107 offset:49152\n\t" \
  "v_mfma_f32_16x16x32_bf16 v[8:11], v[24:27], v[64:67], v[8:11]\n\t" \
  "ds_read_b128 v[80:83], v110 offset:18432\n\t" \
  "ds_read_b128 v[84:87], v110 offset:20480\n\t" \
  "v_mfma_f32_16x16x32_bf16 v[12:15], v[28:31], v[56:59], v[12:15]\n\t" \
  "ds_read_b128 v[88:91], v110 offset:22528\n\t" \
  "ds_read_b64_tr_b16 v[48:49], v104 offset:57344\n\t" \
  "v_mfma_f32_16x16x32_bf16 v[16:19], v[28:31], v[60:63], v[16:19]\n\t" \
  "ds_read_b64_tr_b16 v[50:51], v105 offset:57344\n\t" \
  "ds_read_b64_tr_b16 v[52:53], v106 offset:57344\n\t" \
  "v_mfma_f32_16x16x32_bf16 v[20:23], v[28:31], v[64:67], v[20:23]\n\t" \
  "ds_read_b64_tr_b16 v[54:55], v107 offset:57344\n\t" \
  "ds_read_b128 v[92:95], v111 offset:18432\n\t" \
  "v_mfma_f32_16x16x32_bf16 v[0:3], v[32:35], v[68:71], v[0:3]\n\t" \
  "ds_read_b128 v[96:99], v111 offset:20480\n\t" \
  "ds_read_b128 v[100:103], v111 offset:22528\n\t" \
  "v_mfma_f32_16x16x32_bf16 v[4:7], v[32:35], v[72:75], v[4:7]\n\t" \
  "v_mfma_f32_16x16x32_bf16 v[8:11], v[32:35], v[76:79], v[8:11]\n\t" \
  "v_mfma_f32_16x16x32_bf16 v[12:15], v[36:39], v[68:71], v[12:15]\n\t" \
  "v_mfma_f32_16x16x32_bf16 v[16:19], v[36:39], v[72:75], v[16:19]\n\t" \
  "v_mfma_f32_16x16x32_bf16 v[20:23], v[36:39], v[76:79], v[20:23]\n\t" \
  "s_waitcnt lgkmcnt(0)\n\t" \
  "s_barrier\n\t" \
  "v_mfma_f32_16x16x32_bf16 v[0:3], v[40:43], v[80:83], v[0:3]\n\t" \
  "ds_read_b64_tr_b16 v[24:25], v104 offset:0\n\t" \
  "ds_read_b64_tr_b16 v[26:27], v105 offset:0\n\t" \
  "v_mfma_f32_16x16x32_bf16 v[4:7], v[40:43], v[84:87], v[4:7]\n\t" \
  "ds_read_b64_tr_b16 v[28:29], v106 offset:0\n\t" \
  "ds_read_b64_tr_b16 v[30:31], v107 offset:0\n\t" \
  "v_mfma_f32_16x16x32_bf16 v[8:11], v[40:43], v[88:91], v[8:11]\n\t" \
  "ds_read_b128 v[56:59], v110 offset:24576\n\t" \
  "ds_read_b128 v[60:63], v110 offset:26624\n\t" \
  "v_mfma_f32_16x16x32_bf16 v[12:15], v[44:47], v[80:83], v[12:15]\n\t" \
  "ds_read_b128 v[64:67], v110 offset:28672\n\t" \
  "ds_read_b64_tr_b16 v[32:33], v104 offset:8192\n\t" \
  "v_mfma_f32_16x16x32_bf16 v[16:19], v[44:47], v[84:87], v[16:19]\n\t" \
  "ds_read_b64_tr_b16 v[34:35], v105 offset:8192\n\t" \
  "ds_read_b64_tr_b16 v[36:37], v106 offset:8192\n\t" \
  "v_mfma_f32_16x16x32_bf16 v[20:23], v[44:47], v[88:91], v[20:23]\n\t" \
  "ds_read_b64_tr_b16 v[38:39], v107 offset:8192\n\t" \
  "ds_read_b128 v[68:71], v111 offset:24576\n\t" \
  "v_mfma_f32_16x16x32_bf16 v[0:3], v[48:51], v[92:95], v[0:3]\n\t" \
  "ds_read_b128 v[72:75], v111 offset:26624\n\t" \
  "ds_read_b128 v[76:79], v111 offset:28672\n\t" \
  "v_mfma_f32_16x16x32_bf16 v[4:7], v[48:51], v[96:99], v[4:7]\n\t" \
  "v_mfma_f32_16x16x32_bf16 v[8:11], v[48:51], v[100:103], v[8:11]\n\t" \
  "v_mfma_f32_16x16x32_bf16 v[12:15], v[52:55], v[92:95], v[12:15]\n\t" \
  "v_mfma_f32_16x16x32_bf16 v[16:19], v[52:55], v[96:99], v[16:19]\n\t" \
  "v_mfma_f32_16x16x32_bf16 v[20:23], v[52:55], v[100:103], v[20:23]\n\t" \
  "s_waitcnt lgkmcnt(0)\n\t" \
  "s_barrier\n\t" \
  "v_mfma_f32_16x16x32_bf16 v[0:3], v[24:27], v[56:59], v[0:3]\n\t" \
  "ds_read_b64_tr_b16 v[40:41], v104 offset:16384\n\t" \
  "ds_read_b64_tr_b16 v[42:43], v105 offset:16384\n\t" \
  "v_mfma_f32_16x16x32_bf16 v[4:7], v[24:27], v[60:63], v[4:7]\n\t" \
  "ds_read_b64_tr_b16 v[44:45], v106 offset:16384\n\t" \
  "ds_read_b64_tr_b16 v[46:47], v107 offset:16384\n\t" \
  "v_mfma_f32_16x16x32_bf16 v[8:11], v[24:27], v[64:67], v[8:11]\n\t" \
  "ds_read_b128 v[80:83], v110 offset:30720\n\t" \
  "ds_read_b128 v[84:87], v110 offset:32768\n\t" \
  "v_mfma_f32_16x16x32_bf16 v[12:15], v[28:31], v[56:59], v[12:15]\n\t" \
  "ds_read_b128 v[88:91], v110 offset:34816\n\t" \
  "ds_read_b64_tr_b16 v[48:49], v104 offset:24576\n\t" \
  "v_mfma_f32_16x16x32_bf16 v[16:19], v[28:31], v[60:63], v[16:19]\n\t" \
  "ds_read_b64_tr_b16 v[50:51], v105 offset:24576\n\t" \
  "ds_read_b64_tr_b16 v[52:53], v106 offset:24576\n\t" \
  "v_mfma_f32_16x16x32_bf16 v[20:23], v[28:31], v[64:67], v[20:23]\n\t" \
  "ds_read_b64_tr_b16 v[54:55], v107 offset:24576\n\t" \
  "ds_read_b128 v[92:95], v111 offset:30720\n\t" \
  "v_mfma_f32_16x16x32_bf16 v[0:3], v[32:35], v[68:71], v[0:3]\n\t" \
  "ds_read_b128 v[96:99], v111 offset:32768\n\t" \
  "ds_read_b128 v[100:103], v111 offset:34816\n\t" \
  "v_mfma_f32_16x16x32_bf16 v[4:7], v[32:35], v[72:75], v[4:7]\n\t" \
  "v_mfma_f32_16x16x32_bf16 v[8:11], v[32:35], v[76:79], v[8:11]\n\t" \
  "v_mfma_f32_16x16x32_bf16 v[12:15], v[36:39], v[68:71], v[12:15]\n\t" \
  "v_mfma_f32_16x16x32_bf16 v[16:19], v[36:39], v[72:75], v[16:19]\n\t" \
  "v_mfma_f32_16x16x32_bf16 v[20:23], v[36:39], v[76:79], v[20:23]\n\t" \
  "s_waitcnt lgkmcnt(0)\n\t" \
  "s_barrier\n\t" \
  "v_mfma_f32_16x16x32_bf16 v[0:3], v[40:43], v[80:83], v[0:3]\n\t" \
  "ds_read_b64_tr_b16 v[24:25], v104 offset:32768\n\t" \
  "ds_read_b64_tr_b16 v[26:27], v105 offset:32768\n\t" \
  "v_mfma_f32_16x16x32_bf16 v[4:7], v[40:43], v[84:87], v[4:7]\n\t" \
  "ds_read_b64_tr_b16 v[28:29], v106 offset:32768\n\t" \
  "ds_read_b64_tr_b16 v[30:31], v107 offset:32768\n\t" \
  "v_mfma_f32_16x16x32_bf16 v[8:11], v[40:43], v[88:91], v[8:11]\n\t" \
  "ds_read_b128 v[56:59], v110 offset:36864\n\t" \
  "ds_read_b128 v[60:63], v110 offset:38912\n\t" \
  "v_mfma_f32_16x16x32_bf16 v[12:15], v[44:47], v[80:83], v[12:15]\n\t" \
  "ds_read_b128 v[64:67], v110 offset:40960\n\t" \
  "ds_read_b64_tr_b16 v[32:33], v104 offset:40960\n\t" \
  "v_mfma_f32_16x16x32_bf16 v[16:19], v[44:47], v[84:87], v[16:19]\n\t" \
  "ds_read_b64_tr_b16 v[34:35], v105 offset:40960\n\t" \
  "ds_read_b64_tr_b16 v[36:37], v106 offset:40960\n\t" \
  "v_mfma_f32_16x16x32_bf16 v[20:23], v[44:47], v[88:91], v[20:23]\n\t" \
  "ds_read_b64_tr_b16 v[38:39], v107 offset:40960\n\t" \
  "ds_read_b128 v[68:71], v111 offset:36864\n\t" \
  "v_mfma_f32_16x16x32_bf16 v[0:3], v[48:51], v[92:95], v[0:3]\n\t" \
  "ds_read_b128 v[72:75], v111 offset:38912\n\t" \
  "ds_read_b128 v[76:79], v111 offset:40960\n\t" \
  "v_mfma_f32_16x16x32_bf16 v[4:7], v[48:51], v[96:99], v[4:7]\n\t" \
  "v_mfma_f32_16x16x32_bf16 v[8:11], v[48:51], v[100:103], v[8:11]\n\t" \
  "v_mfma_f32_16x16x32_bf16 v[12:15], v[52:55], v[92:95], v[12:15]\n\t" \
  "v_mfma_f32_16x16x32_bf16 v[16:19], v[52:55], v[96:99], v[16:19]\n\t" \
  "v_mfma_f32_16x16x32_bf16 v[20:23], v[52:55], v[100:103], v[20:23]\n\t" \
  "s_waitcnt lgkmcnt(0)\n\t" \
  "s_barrier\n\t" \
  "v_mfma_f32_16x16x32_bf16 v[0:3], v[24:27], v[56:59], v[0:3]\n\t" \
  "ds_read_b64_tr_b16 v[40:41], v104 offset:49152\n\t" \
  "ds_read_b64_tr_b16 v[42:43], v105 offset:49152\n\t" \
  "v_mfma_f32_16x16x32_bf16 v[4:7], v[24:27], v[60:63], v[4:7]\n\t" \
  "ds_read_b64_tr_b16 v[44:45], v106 offset:49152\n\t" \
  "ds_read_b64_tr_b16 v[46:47], v107 offset:49152\n\t" \
  "v_mfma_f32_16x16x32_bf16 v[8:11], v[24:27], v[64:67], v[8:11]\n\t" \
  "ds_read_b128 v[80:83], v110 offset:43008\n\t" \
  "ds_read_b128 v[84:87], v110 offset:45056\n\t" \
  "v_mfma_f32_16x16x32_bf16 v[12:15], v[28:31], v[56:59], v[12:15]\n\t" \
  "ds_read_b128 v[88:91], v110 offset:47104\n\t" \
  "ds_read_b64_tr_b16 v[48:49], v104 offset:57344\n\t" \
  "v_mfma_f32_16x16x32_bf16 v[16:19], v[28:31], v[60:63], v[16:19]\n\t" \
  "ds_read_b64_tr_b16 v[50:51], v105 offset:57344\n\t" \
  "ds_read_b64_tr_b16 v[52:53], v106 offset:57344\n\t" \
  "v_mfma_f32_16x16x32_bf16 v[20:23], v[28:31], v[64:67], v[20:23]\n\t" \
  "ds_read_b64_tr_b16 v[54:55], v107 offset:57344\n\t" \
  "ds_read_b128 v[92:95], v111 offset:43008\n\t" \
  "v_mfma_f32_16x16x32_bf16 v[0:3], v[32:35], v[68:71], v[0:3]\n\t" \
  "ds_read_b128 v[96:99], v111 offset:45056\n\t" \
  "ds_read_b128 v[100:103], v111 offset:47104\n\t" \
  "v_mfma_f32_16x16x32_bf16 v[4:7], v[32:35], v[72:75], v[4:7]\n\t" \
  "v_mfma_f32_16x16x32_bf16 v[8:11], v[32:35], v[76:79], v[8:11]\n\t" \
  "v_mfma_f32_16x16x32_bf16 v[12:15], v[36:39], v[68:71], v[12:15]\n\t" \
  "v_mfma_f32_16x16x32_bf16 v[16:19], v[36:39], v[72:75], v[16:19]\n\t" \
  "v_mfma_f32_16x16x32_bf16 v[20:23], v[36:39], v[76:79], v[20:23]\n\t" \
  "s_waitcnt lgkmcnt(9)\n\t" \
  "v_mfma_f32_16x16x32_bf16 v[0:3], v[40:43], v[80:83], v[0:3]\n\t" \
  "s_waitcnt lgkmcnt(8)\n\t" \
  "v_mfma_f32_16x16x32_bf16 v[4:7], v[40:43], v[84:87], v[4:7]\n\t" \
  "s_waitcnt lgkmcnt(7)\n\t" \
  "v_mfma_f32_16x16x32_bf16 v[8:11], v[40:43], v[88:91], v[8:11]\n\t" \
  "v_mfma_f32_16x16x32_bf16 v[12:15], v[44:47], v[80:83], v[12:15]\n\t" \
  "v_mfma_f32_16x16x32_bf16 v[16:19], v[44:47], v[84:87], v[16:19]\n\t" \
  "v_mfma_f32_16x16x32_bf16 v[20:23], v[44:47], v[88:91], v[20:23]\n\t" \
  "s_waitcnt lgkmcnt(2)\n\t" \
  "v_mfma_f32_16x16x32_bf16 v[0:3], v[48:51], v[92:95], v[0:3]\n\t" \
  "s_waitcnt lgkmcnt(1)\n\t" \
  "v_mfma_f32_16x16x32_bf16 v[4:7], v[48:51], v[96:99], v[4:7]\n\t" \
  "s_waitcnt lgkmcnt(0)\n\t" \
  "v_mfma_f32_16x16x32_bf16 v[8:11], v[48:51], v[100:103], v[8:11]\n\t" \
  "v_mfma_f32_16x16x32_bf16 v[12:15], v[52:55], v[92:95], v[12:15]\n\t" \
  "v_mfma_f32_16x16x32_bf16 v[16:19], v[52:55], v[96:99], v[16:19]\n\t" \
  "v_mfma_f32_16x16x32_bf16 v[20:23], v[52:55], v[100:103], v[20:23]\n\t" \
  "s_nop 7\n\t" \
  "s_nop 1\n\t"
#define GEMM_O_ASM \
  "s_barrier\n\t" \
  "ds_read_b64_tr_b16 v[24:25], v104 offset:0\n\t" \
  "ds_read_b64_tr_b16 v[26:27], v105 offset:0\n\t" \
  "ds_read_b64_tr_b16 v[28:29], v106 offset:0\n\t" \
  "ds_read_b64_tr_b16 v[30:31], v107 offset:0\n\t" \
  "ds_read_b128 v[56:59], v108 offset:0\n\t" \
  "ds_read_b128 v[60:63], v108 offset:1024\n\t" \
  "ds_read_b128 v[64:67], v108 offset:2048\n\t" \
  "ds_read_b64_tr_b16 v[32:33], v104 offset:8192\n\t" \
  "ds_read_b64_tr_b16 v[34:35], v105 offset:8192\n\t" \
  "ds_read_b64_tr_b16 v[36:37], v106 offset:8192\n\t" \
  "ds_read_b64_tr_b16 v[38:39], v107 offset:8192\n\t" \
  "ds_read_b128 v[68:71], v109 offset:0\n\t" \
  "ds_read_b128 v[72:75], v109 offset:1024\n\t" \
  "ds_read_b128 v[76:79], v109 offset:2048\n\t" \
  "s_waitcnt lgkmcnt(0)\n\t" \
  "s_barrier\n\t" \
  "v_mfma_f32_16x16x32_bf16 v[0:3], v[24:27], v[56:59], 0\n\t" \
  "ds_read_b64_tr_b16 v[40:41], v104 offset:16384\n\t" \
  "ds_read_b64_tr_b16 v[42:43], v105 offset:16384\n\t" \
  "v_mfma_f32_16x16x32_bf16 v[4:7], v[24:27], v[60:63], 0\n\t" \
  "ds_read_b64_tr_b16 v[44:45], v106 offset:16384\n\t" \
  "ds_read_b64_tr_b16 v[46:47], v107 offset:16384\n\t" \
  "v_mfma_f32_16x16x32_bf16 v[8:11], v[24:27], v[64:67], 0\n\t" \
  "ds_read_b128 v[80:83], v108 offset:6144\n\t" \
  "ds_read_b128 v[84:87], v108 offset:7168\n\t" \
  "v_mfma_f32_16x16x32_bf16 v[12:15], v[28:31], v[56:59], 0\n\t" \
  "ds_read_b128 v[88:91], v108 offset:8192\n\t" \
  "ds_read_b64_tr_b16 v[48:49], v104 offset:24576\n\t" \
  "v_mfma_f32_16x16x32_bf16 v[16:19], v[28:31], v[60:63], 0\n\t" \
  "ds_read_b64_tr_b16 v[50:51], v105 offset:24576\n\t" \
  "ds_read_b64_tr_b16 v[52:53], v106 offset:24576\n\t" \
  "v_mfma_f32_16x16x32_bf16 v[20:23], v[28:31], v[64:67], 0\n\t" \
  "ds_read_b64_tr_b16 v[54:55], v107 offset:24576\n\t" \
  "ds_read_b128 v[92:95], v109 offset:6144\n\t" \
  "v_mfma_f32_16x16x32_bf16 v[0:3], v[32:35], v[68:71], v[0:3]\n\t" \
  "ds_read_b128 v[96:99], v109 offset:7168\n\t" \
  "ds_read_b128 v[100:103], v109 offset:8192\n\t" \
  "v_mfma_f32_16x16x32_bf16 v[4:7], v[32:35], v[72:75], v[4:7]\n\t" \
  "v_mfma_f32_16x16x32_bf16 v[8:11], v[32:35], v[76:79], v[8:11]\n\t" \
  "v_mfma_f32_16x16x32_bf16 v[12:15], v[36:39], v[68:71], v[12:15]\n\t" \
  "v_mfma_f32_16x16x32_bf16 v[16:19], v[36:39], v[72:75], v[16:19]\n\t" \
  "v_mfma_f32_16x16x32_bf16 v[20:23], v[36:39], v[76:79], v[20:23]\n\t" \
  "s_waitcnt lgkmcnt(0)\n\t" \
  "s_barrier\n\t" \
  "v_mfma_f32_16x16x32_bf16 v[0:3], v[40:43], v[80:83], v[0:3]\n\t" \
  "ds_read_b64_tr_b16 v[24:25], v104 offset:32768\n\t" \
  "ds_read_b64_tr_b16 v[26:27], v105 offset:32768\n\t" \
  "v_mfma_f32_16x16x32_bf16 v[4:7], v[40:43], v[84:87], v[4:7]\n\t" \
  "ds_read_b64_tr_b16 v[28:29], v106 offset:32768\n\t" \
  "ds_read_b64_tr_b16 v[30:31], v107 offset:32768\n\t" \
  "v_mfma_f32_16x16x32_bf16 v[8:11], v[40:43], v[88:91], v[8:11]\n\t" \
  "ds_read_b128 v[56:59], v108 offset:12288\n\t" \
  "ds_read_b128 v[60:63], v108 offset:13312\n\t" \
  "v_mfma_f32_16x16x32_bf16 v[12:15], v[44:47], v[80:83], v[12:15]\n\t" \
  "ds_read_b128 v[64:67], v108 offset:14336\n\t" \
  "ds_read_b64_tr_b16 v[32:33], v104 offset:40960\n\t" \
  "v_mfma_f32_16x16x32_bf16 v[16:19], v[44:47], v[84:87], v[16:19]\n\t" \
  "ds_read_b64_tr_b16 v[34:35], v105 offset:40960\n\t" \
  "ds_read_b64_tr_b16 v[36:37], v106 offset:40960\n\t" \
  "v_mfma_f32_16x16x32_bf16 v[20:23], v[44:47], v[88:91], v[20:23]\n\t" \
  "ds_read_b64_tr_b16 v[38:39], v107 offset:40960\n\t" \
  "ds_read_b128 v[68:71], v109 offset:12288\n\t" \
  "v_mfma_f32_16x16x32_bf16 v[0:3], v[48:51], v[92:95], v[0:3]\n\t" \
  "ds_read_b128 v[72:75], v109 offset:13312\n\t" \
  "ds_read_b128 v[76:79], v109 offset:14336\n\t" \
  "v_mfma_f32_16x16x32_bf16 v[4:7], v[48:51], v[96:99], v[4:7]\n\t" \
  "v_mfma_f32_16x16x32_bf16 v[8:11], v[48:51], v[100:103], v[8:11]\n\t" \
  "v_mfma_f32_16x16x32_bf16 v[12:15], v[52:55], v[92:95], v[12:15]\n\t" \
  "v_mfma_f32_16x16x32_bf16 v[16:19], v[52:55], v[96:99], v[16:19]\n\t" \
  "v_mfma_f32_16x16x32_bf16 v[20:23], v[52:55], v[100:103], v[20:23]\n\t" \
  "s_waitcnt lgkmcnt(0)\n\t" \
  "s_barrier\n\t" \
  "v_mfma_f32_16x16x32_bf16 v[0:3], v[24:27], v[56:59], v[0:3]\n\t" \
  "ds_read_b64_tr_b16 v[40:41], v104 offset:49152\n\t" \
  "ds_read_b64_tr_b16 v[42:43], v105 offset:49152\n\t" \
  "v_mfma_f32_16x16x32_bf16 v[4:7], v[24:27], v[60:63], v[4:7]\n\t" \
  "ds_read_b64_tr_b16 v[44:45], v106 offset:49152\n\t" \
  "ds_read_b64_tr_b16 v[46:47], v107 offset:49152\n\t" \
  "v_mfma_f32_16x16x32_bf16 v[8:11], v[24:27], v[64:67], v[8:11]\n\t" \
  "ds_read_b128 v[80:83], v108 offset:18432\n\t" \
  "ds_read_b128 v[84:87], v108 offset:19456\n\t" \
  "v_mfma_f32_16x16x32_bf16 v[12:15], v[28:31], v[56:59], v[12:15]\n\t" \
  "ds_read_b128 v[88:91], v108 offset:20480\n\t" \
  "ds_read_b64_tr_b16 v[48:49], v104 offset:57344\n\t" \
  "v_mfma_f32_16x16x32_bf16 v[16:19], v[28:31], v[60:63], v[16:19]\n\t" \
  "ds_read_b64_tr_b16 v[50:51], v105 offset:57344\n\t" \
  "ds_read_b64_tr_b16 v[52:53], v106 offset:57344\n\t" \
  "v_mfma_f32_16x16x32_bf16 v[20:23], v[28:31], v[64:67], v[20:23]\n\t" \
  "ds_read_b64_tr_b16 v[54:55], v107 offset:57344\n\t" \
  "ds_read_b128 v[92:95], v109 offset:18432\n\t" \
  "v_mfma_f32_16x16x32_bf16 v[0:3], v[32:35], v[68:71], v[0:3]\n\t" \
  "ds_read_b128 v[96:99], v109 offset:19456\n\t" \
  "ds_read_b128 v[100:103], v109 offset:20480\n\t" \
  "v_mfma_f32_16x16x32_bf16 v[4:7], v[32:35], v[72:75], v[4:7]\n\t" \
  "v_mfma_f32_16x16x32_bf16 v[8:11], v[32:35], v[76:79], v[8:11]\n\t" \
  "v_mfma_f32_16x16x32_bf16 v[12:15], v[36:39], v[68:71], v[12:15]\n\t" \
  "v_mfma_f32_16x16x32_bf16 v[16:19], v[36:39], v[72:75], v[16:19]\n\t" \
  "v_mfma_f32_16x16x32_bf16 v[20:23], v[36:39], v[76:79], v[20:23]\n\t" \
  "s_waitcnt lgkmcnt(0)\n\t" \
  "s_barrier\n\t" \
  "v_mfma_f32_16x16x32_bf16 v[0:3], v[40:43], v[80:83], v[0:3]\n\t" \
  "ds_read_b64_tr_b16 v[24:25], v112 offset:0\n\t" \
  "ds_read_b64_tr_b16 v[26:27], v113 offset:0\n\t" \
  "v_mfma_f32_16x16x32_bf16 v[4:7], v[40:43], v[84:87], v[4:7]\n\t" \
  "ds_read_b64_tr_b16 v[28:29], v114 offset:0\n\t" \
  "ds_read_b64_tr_b16 v[30:31], v115 offset:0\n\t" \
  "v_mfma_f32_16x16x32_bf16 v[8:11], v[40:43], v[88:91], v[8:11]\n\t" \
  "ds_read_b128 v[56:59], v108 offset:24576\n\t" \
  "ds_read_b128 v[60:63], v108 offset:25600\n\t" \
  "v_mfma_f32_16x16x32_bf16 v[12:15], v[44:47], v[80:83], v[12:15]\n\t" \
  "ds_read_b128 v[64:67], v108 offset:26624\n\t" \
  "ds_read_b64_tr_b16 v[32:33], v112 offset:8192\n\t" \
  "v_mfma_f32_16x16x32_bf16 v[16:19], v[44:47], v[84:87], v[16:19]\n\t" \
  "ds_read_b64_tr_b16 v[34:35], v113 offset:8192\n\t" \
  "ds_read_b64_tr_b16 v[36:37], v114 offset:8192\n\t" \
  "v_mfma_f32_16x16x32_bf16 v[20:23], v[44:47], v[88:91], v[20:23]\n\t" \
  "ds_read_b64_tr_b16 v[38:39], v115 offset:8192\n\t" \
  "ds_read_b128 v[68:71], v109 offset:24576\n\t" \
  "v_mfma_f32_16x16x32_bf16 v[0:3], v[48:51], v[92:95], v[0:3]\n\t" \
  "ds_read_b128 v[72:75], v109 offset:25600\n\t" \
  "ds_read_b128 v[76:79], v109 offset:26624\n\t" \
  "v_mfma_f32_16x16x32_bf16 v[4:7], v[48:51], v[96:99], v[4:7]\n\t" \
  "v_mfma_f32_16x16x32_bf16 v[8:11], v[48:51], v[100:103], v[8:11]\n\t" \
  "v_mfma_f32_16x16x32_bf16 v[12:15], v[52:55], v[92:95], v[12:15]\n\t" \
  "v_mfma_f32_16x16x32_bf16 v[16:19], v[52:55], v[96:99], v[16:19]\n\t" \
  "v_mfma_f32_16x16x32_bf16 v[20:23], v[52:55], v[100:103], v[20:23]\n\t" \
  "s_waitcnt lgkmcnt(0)\n\t" \
  "s_barrier\n\t" \
  "v_mfma_f32_16x16x32_bf16 v[0:3], v[24:27], v[56:59], v[0:3]\n\t" \
  "ds_read_b64_tr_b16 v[40:41], v112 offset:16384\n\t" \
  "ds_read_b64_tr_b16 v[42:43], v113 offset:16384\n\t" \
  "v_mfma_f32_16x16x32_bf16 v[4:7], v[24:27], v[60:63], v[4:7]\n\t" \
  "ds_read_b64_tr_b16 v[44:45], v114 offset:16384\n\t" \
  "ds_read_b64_tr_b16 v[46:47], v115 offset:16384\n\t" \
  "v_mfma_f32_16x16x32_bf16 v[8:11], v[24:27], v[64:67], v[8:11]\n\t" \
  "ds_read_b128 v[80:83], v108 offset:30720\n\t" \
  "ds_read_b128 v[84:87], v108 offset:31744\n\t" \
  "v_mfma_f32_16x16x32_bf16 v[12:15], v[28:31], v[56:59], v[12:15]\n\t" \
  "ds_read_b128 v[88:91], v108 offset:32768\n\t" \
  "ds_read_b64_tr_b16 v[48:49], v112 offset:24576\n\t" \
  "v_mfma_f32_16x16x32_bf16 v[16:19], v[28:31], v[60:63], v[16:19]\n\t" \
  "ds_read_b64_tr_b16 v[50:51], v113 offset:24576\n\t" \
  "ds_read_b64_tr_b16 v[52:53], v114 offset:24576\n\t" \
  "v_mfma_f32_16x16x32_bf16 v[20:23], v[28:31], v[64:67], v[20:23]\n\t" \
  "ds_read_b64_tr_b16 v[54:55], v115 offset:24576\n\t" \
  "ds_read_b128 v[92:95], v109 offset:30720\n\t" \
  "v_mfma_f32_16x16x32_bf16 v[0:3], v[32:35], v[68:71], v[0:3]\n\t" \
  "ds_read_b128 v[96:99], v109 offset:31744\n\t" \
  "ds_read_b128 v[100:103], v109 offset:32768\n\t" \
  "v_mfma_f32_16x16x32_bf16 v[4:7], v[32:35], v[72:75], v[4:7]\n\t" \
  "v_mfma_f32_16x16x32_bf16 v[8:11], v[32:35], v[76:79], v[8:11]\n\t" \
  "v_mfma_f32_16x16x32_bf16 v[12:15], v[36:39], v[68:71], v[12:15]\n\t" \
  "v_mfma_f32_16x16x32_bf16 v[16:19], v[36:39], v[72:75], v[16:19]\n\t" \
  "v_mfma_f32_16x16x32_bf16 v[20:23], v[36:39], v[76:79], v[20:23]\n\t" \
  "s_waitcnt lgkmcnt(0)\n\t" \
  "s_barrier\n\t" \
  "v_mfma_f32_16x16x32_bf16 v[0:3], v[40:43], v[80:83], v[0:3]\n\t" \
  "ds_read_b64_tr_b16 v[24:25], v112 offset:32768\n\t" \
  "ds_read_b64_tr_b16 v[26:27], v113 offset:32768\n\t" \
  "v_mfma_f32_16x16x32_bf16 v[4:7], v[40:43], v[84:87], v[4:7]\n\t" \
  "ds_read_b64_tr_b16 v[28:29], v114 offset:32768\n\t" \
  "ds_read_b64_tr_b16 v[30:31], v115 offset:32768\n\t" \
  "v_mfma_f32_16x16x32_bf16 v[8:11], v[40:43], v[88:91], v[8:11]\n\t" \
  "ds_read_b128 v[56:59], v108 offset:36864\n\t" \
  "ds_read_b128 v[60:63], v108 offset:37888\n\t" \
  "v_mfma_f32_16x16x32_bf16 v[12:15], v[44:47], v[80:83], v[12:15]\n\t" \
  "ds_read_b128 v[64:67], v108 offset:38912\n\t" \
  "ds_read_b64_tr_b16 v[32:33], v112 offset:40960\n\t" \
  "v_mfma_f32_16x16x32_bf16 v[16:19], v[44:47], v[84:87], v[16:19]\n\t" \
  "ds_read_b64_tr_b16 v[34:35], v113 offset:40960\n\t" \
  "ds_read_b64_tr_b16 v[36:37], v114 offset:40960\n\t" \
  "v_mfma_f32_16x16x32_bf16 v[20:23], v[44:47], v[88:91], v[20:23]\n\t" \
  "ds_read_b64_tr_b16 v[38:39], v115 offset:40960\n\t" \
  "ds_read_b128 v[68:71], v109 offset:36864\n\t" \
  "v_mfma_f32_16x16x32_bf16 v[0:3], v[48:51], v[92:95], v[0:3]\n\t" \
  "ds_read_b128 v[72:75], v109 offset:37888\n\t" \
  "ds_read_b128 v[76:79], v109 offset:38912\n\t" \
  "v_mfma_f32_16x16x32_bf16 v[4:7], v[48:51], v[96:99], v[4:7]\n\t" \
  "v_mfma_f32_16x16x32_bf16 v[8:11], v[48:51], v[100:103], v[8:11]\n\t" \
  "v_mfma_f32_16x16x32_bf16 v[12:15], v[52:55], v[92:95], v[12:15]\n\t" \
  "v_mfma_f32_16x16x32_bf16 v[16:19], v[52:55], v[96:99], v[16:19]\n\t" \
  "v_mfma_f32_16x16x32_bf16 v[20:23], v[52:55], v[100:103], v[20:23]\n\t" \
  "s_waitcnt lgkmcnt(0)\n\t" \
  "s_barrier\n\t" \
  "v_mfma_f32_16x16x32_bf16 v[0:3], v[24:27], v[56:59], v[0:3]\n\t" \
  "ds_read_b64_tr_b16 v[40:41], v104 offset:0\n\t" \
  "ds_read_b64_tr_b16 v[42:43], v105 offset:0\n\t" \
  "v_mfma_f32_16x16x32_bf16 v[4:7], v[24:27], v[60:63], v[4:7]\n\t" \
  "ds_read_b64_tr_b16 v[44:45], v106 offset:0\n\t" \
  "ds_read_b64_tr_b16 v[46:47], v107 offset:0\n\t" \
  "v_mfma_f32_16x16x32_bf16 v[8:11], v[24:27], v[64:67], v[8:11]\n\t" \
  "ds_read_b128 v[80:83], v108 offset:43008\n\t" \
  "ds_read_b128 v[84:87], v108 offset:44032\n\t" \
  "v_mfma_f32_16x16x32_bf16 v[12:15], v[28:31], v[56:59], v[12:15]\n\t" \
  "ds_read_b128 v[88:91], v108 offset:45056\n\t" \
  "ds_read_b64_tr_b16 v[48:49], v104 offset:8192\n\t" \
  "v_mfma_f32_16x16x32_bf16 v[16:19], v[28:31], v[60:63], v[16:19]\n\t" \
  "ds_read_b64_tr_b16 v[50:51], v105 offset:8192\n\t" \
  "ds_read_b64_tr_b16 v[52:53], v106 offset:8192\n\t" \
  "v_mfma_f32_16x16x32_bf16 v[20:23], v[28:31], v[64:67], v[20:23]\n\t" \
  "ds_read_b64_tr_b16 v[54:55], v107 offset:8192\n\t" \
  "ds_read_b128 v[92:95], v109 offset:43008\n\t" \
  "v_mfma_f32_16x16x32_bf16 v[0:3], v[32:35], v[68:71], v[0:3]\n\t" \
  "ds_read_b128 v[96:99], v109 offset:44032\n\t" \
  "ds_read_b128 v[100:103], v109 offset:45056\n\t" \
  "v_mfma_f32_16x16x32_bf16 v[4:7], v[32:35], v[72:75], v[4:7]\n\t" \
  "v_mfma_f32_16x16x32_bf16 v[8:11], v[32:35], v[76:79], v[8:11]\n\t" \
  "v_mfma_f32_16x16x32_bf16 v[12:15], v[36:39], v[68:71], v[12:15]\n\t" \
  "v_mfma_f32_16x16x32_bf16 v[16:19], v[36:39], v[72:75], v[16:19]\n\t" \
  "v_mfma_f32_16x16x32_bf16 v[20:23], v[36:39], v[76:79], v[20:23]\n\t" \
  "s_waitcnt lgkmcnt(0)\n\t" \
  "s_barrier\n\t" \
  "v_mfma_f32_16x16x32_bf16 v[0:3], v[40:43], v[80:83], v[0:3]\n\t" \
  "ds_read_b64_tr_b16 v[24:25], v104 offset:16384\n\t" \
  "ds_read_b64_tr_b16 v[26:27], v105 offset:16384\n\t" \
  "v_mfma_f32_16x16x32_bf16 v[4:7], v[40:43], v[84:87], v[4:7]\n\t" \
  "ds_read_b64_tr_b16 v[28:29], v106 offset:16384\n\t" \
  "ds_read_b64_tr_b16 v[30:31], v107 offset:16384\n\t" \
  "v_mfma_f32_16x16x32_bf16 v[8:11], v[40:43], v[88:91], v[8:11]\n\t" \
  "ds_read_b128 v[56:59], v108 offset:0\n\t" \
  "ds_read_b128 v[60:63], v108 offset:1024\n\t" \
  "v_mfma_f32_16x16x32_bf16 v[12:15], v[44:47], v[80:83], v[12:15]\n\t" \
  "ds_read_b128 v[64:67], v108 offset:2048\n\t" \
  "ds_read_b64_tr_b16 v[32:33], v104 offset:24576\n\t" \
  "v_mfma_f32_16x16x32_bf16 v[16:19], v[44:47], v[84:87], v[16:19]\n\t" \
  "ds_read_b64_tr_b16 v[34:35], v105 offset:24576\n\t" \
  "ds_read_b64_tr_b16 v[36:37], v106 offset:24576\n\t" \
  "v_mfma_f32_16x16x32_bf16 v[20:23], v[44:47], v[88:91], v[20:23]\n\t" \
  "ds_read_b64_tr_b16 v[38:39], v107 offset:24576\n\t" \
  "ds_read_b128 v[68:71], v109 offset:0\n\t" \
  "v_mfma_f32_16x16x32_bf16 v[0:3], v[48:51], v[92:95], v[0:3]\n\t" \
  "ds_read_b128 v[72:75], v109 offset:1024\n\t" \
  "ds_read_b128 v[76:79], v109 offset:2048\n\t" \
  "v_mfma_f32_16x16x32_bf16 v[4:7], v[48:51], v[96:99], v[4:7]\n\t" \
  "v_mfma_f32_16x16x32_bf16 v[8:11], v[48:51], v[100:103], v[8:11]\n\t" \
  "v_mfma_f32_16x16x32_bf16 v[12:15], v[52:55], v[92:95], v[12:15]\n\t" \
  "v_mfma_f32_16x16x32_bf16 v[16:19], v[52:55], v[96:99], v[16:19]\n\t" \
  "v_mfma_f32_16x16x32_bf16 v[20:23], v[52:55], v[100:103], v[20:23]\n\t" \
  "s_waitcnt lgkmcnt(0)\n\t" \
  "s_barrier\n\t" \
  "v_mfma_f32_16x16x32_bf16 v[0:3], v[24:27], v[56:59], v[0:3]\n\t" \
  "ds_read_b64_tr_b16 v[40:41], v104 offset:32768\n\t" \
  "ds_read_b64_tr_b16 v[42:43], v105 offset:32768\n\t" \
  "v_mfma_f32_16x16x32_bf16 v[4:7], v[24:27], v[60:63], v[4:7]\n\t" \
  "ds_read_b64_tr_b16 v[44:45], v106 offset:32768\n\t" \
  "ds_read_b64_tr_b16 v[46:47], v107 offset:32768\n\t" \
  "v_mfma_f32_16x16x32_bf16 v[8:11], v[24:27], v[64:67], v[8:11]\n\t" \
  "ds_read_b128 v[80:83], v108 offset:6144\n\t" \
  "ds_read_b128 v[84:87], v108 offset:7168\n\t" \
  "v_mfma_f32_16x16x32_bf16 v[12:15], v[28:31], v[56:59], v[12:15]\n\t" \
  "ds_read_b128 v[88:91], v108 offset:8192\n\t" \
  "ds_read_b64_tr_b16 v[48:49], v104 offset:40960\n\t" \
  "v_mfma_f32_16x16x32_bf16 v[16:19], v[28:31], v[60:63], v[16:19]\n\t" \
  "ds_read_b64_tr_b16 v[50:51], v105 offset:40960\n\t" \
  "ds_read_b64_tr_b16 v[52:53], v106 offset:40960\n\t" \
  "v_mfma_f32_16x16x32_bf16 v[20:23], v[28:31], v[64:67], v[20:23]\n\t" \
  "ds_read_b64_tr_b16 v[54:55], v107 offset:40960\n\t" \
  "ds_read_b128 v[92:95], v109 offset:6144\n\t" \
  "v_mfma_f32_16x16x32_bf16 v[0:3], v[32:35], v[68:71], v[0:3]\n\t" \
  "ds_read_b128 v[96:99], v109 offset:7168\n\t" \
  "ds_read_b128 v[100:103], v109 offset:8192\n\t" \
  "v_mfma_f32_16x16x32_bf16 v[4:7], v[32:35], v[72:75], v[4:7]\n\t" \
  "v_mfma_f32_16x16x32_bf16 v[8:11], v[32:35], v[76:79], v[8:11]\n\t" \
  "v_mfma_f32_16x16x32_bf16 v[12:15], v[36:39], v[68:71], v[12:15]\n\t" \
  "v_mfma_f32_16x16x32_bf16 v[16:19], v[36:39], v[72:75], v[16:19]\n\t" \
  "v_mfma_f32_16x16x32_bf16 v[20:23], v[36:39], v[76:79], v[20:23]\n\t" \
  "s_waitcnt lgkmcnt(0)\n\t" \
  "s_barrier\n\t" \
  "v_mfma_f32_16x16x32_bf16 v[0:3], v[40:43], v[80:83], v[0:3]\n\t" \
  "ds_read_b64_tr_b16 v[24:25], v104 offset:49152\n\t" \
  "ds_read_b64_tr_b16 v[26:27], v105 offset:49152\n\t" \
  "v_mfma_f32_16x16x32_bf16 v[4:7], v[40:43], v[84:87], v[4:7]\n\t" \
  "ds_read_b64_tr_b16 v[28:29], v106 offset:49152\n\t" \
  "ds_read_b64_tr_b16 v[30:31], v107 offset:49152\n\t" \
  "v_mfma_f32_16x16x32_bf16 v[8:11], v[40:43], v[88:91], v[8:11]\n\t" \
  "ds_read_b128 v[56:59], v108 offset:12288\n\t" \
  "ds_read_b128 v[60:63], v108 offset:13312\n\t" \
  "v_mfma_f32_16x16x32_bf16 v[12:15], v[44:47], v[80:83], v[12:15]\n\t" \
  "ds_read_b128 v[64:67], v108 offset:14336\n\t" \
  "ds_read_b64_tr_b16 v[32:33], v104 offset:57344\n\t" \
  "v_mfma_f32_16x16x32_bf16 v[16:19], v[44:47], v[84:87], v[16:19]\n\t" \
  "ds_read_b64_tr_b16 v[34:35], v105 offset:57344\n\t" \
  "ds_read_b64_tr_b16 v[36:37], v106 offset:57344\n\t" \
  "v_mfma_f32_16x16x32_bf16 v[20:23], v[44:47], v[88:91], v[20:23]\n\t" \
  "ds_read_b64_tr_b16 v[38:39], v107 offset:57344\n\t" \
  "ds_read_b128 v[68:71], v109 offset:12288\n\t" \
  "v_mfma_f32_16x16x32_bf16 v[0:3], v[48:51], v[92:95], v[0:3]\n\t" \
  "ds_read_b128 v[72:75], v109 offset:13312\n\t" \
  "ds_read_b128 v[76:79], v109 offset:14336\n\t" \
  "v_mfma_f32_16x16x32_bf16 v[4:7], v[48:51], v[96:99], v[4:7]\n\t" \
  "v_mfma_f32_16x16x32_bf16 v[8:11], v[48:51], v[100:103], v[8:11]\n\t" \
  "v_mfma_f32_16x16x32_bf16 v[12:15], v[52:55], v[92:95], v[12:15]\n\t" \
  "v_mfma_f32_16x16x32_bf16 v[16:19], v[52:55], v[96:99], v[16:19]\n\t" \
  "v_mfma_f32_16x16x32_bf16 v[20:23], v[52:55], v[100:103], v[20:23]\n\t" \
  "s_waitcnt lgkmcnt(0)\n\t" \
  "s_barrier\n\t" \
  "v_mfma_f32_16x16x32_bf16 v[0:3], v[24:27], v[56:59], v[0:3]\n\t" \
  "ds_read_b64_tr_b16 v[40:41], v112 offset:0\n\t" \
  "ds_read_b64_tr_b16 v[42:43], v113 offset:0\n\t" \
  "v_mfma_f32_16x16x32_bf16 v[4:7], v[24:27], v[60:63], v[4:7]\n\t" \
  "ds_read_b64_tr_b16 v[44:45], v114 offset:0\n\t" \
  "ds_read_b64_tr_b16 v[46:47], v115 offset:0\n\t" \
  "v_mfma_f32_16x16x32_bf16 v[8:11], v[24:27], v[64:67], v[8:11]\n\t" \
  "ds_read_b128 v[80:83], v108 offset:18432\n\t" \
  "ds_read_b128 v[84:87], v108 offset:19456\n\t" \
  "v_mfma_f32_16x16x32_bf16 v[12:15], v[28:31], v[56:59], v[12:15]\n\t" \
  "ds_read_b128 v[88:91], v108 offset:20480\n\t" \
  "ds_read_b64_tr_b16 v[48:49], v112 offset:8192\n\t" \
  "v_mfma_f32_16x16x32_bf16 v[16:19], v[28:31], v[60:63], v[16:19]\n\t" \
  "ds_read_b64_tr_b16 v[50:51], v113 offset:8192\n\t" \
  "ds_read_b64_tr_b16 v[52:53], v114 offset:8192\n\t" \
  "v_mfma_f32_16x16x32_bf16 v[20:23], v[28:31], v[64:67], v[20:23]\n\t" \
  "ds_read_b64_tr_b16 v[54:55], v115 offset:8192\n\t" \
  "ds_read_b128 v[92:95], v109 offset:18432\n\t" \
  "v_mfma_f32_16x16x32_bf16 v[0:3], v[32:35], v[68:71], v[0:3]\n\t" \
  "ds_read_b128 v[96:99], v109 offset:19456\n\t" \
  "ds_read_b128 v[100:103], v109 offset:20480\n\t" \
  "v_mfma_f32_16x16x32_bf16 v[4:7], v[32:35], v[72:75], v[4:7]\n\t" \
  "v_mfma_f32_16x16x32_bf16 v[8:11], v[32:35], v[76:79], v[8:11]\n\t" \
  "v_mfma_f32_16x16x32_bf16 v[12:15], v[36:39], v[68:71], v[12:15]\n\t" \
  "v_mfma_f32_16x16x32_bf16 v[16:19], v[36:39], v[72:75], v[16:19]\n\t" \
  "v_mfma_f32_16x16x32_bf16 v[20:23], v[36:39], v[76:79], v[20:23]\n\t" \
  "s_waitcnt lgkmcnt(0)\n\t" \
  "s_barrier\n\t" \
  "v_mfma_f32_16x16x32_bf16 v[0:3], v[40:43], v[80:83], v[0:3]\n\t" \
  "ds_read_b64_tr_b16 v[24:25], v112 offset:16384\n\t" \
  "ds_read_b64_tr_b16 v[26:27], v113 offset:16384\n\t" \
  "v_mfma_f32_16x16x32_bf16 v[4:7], v[40:43], v[84:87], v[4:7]\n\t" \
  "ds_read_b64_tr_b16 v[28:29], v114 offset:16384\n\t" \
  "ds_read_b64_tr_b16 v[30:31], v115 offset:16384\n\t" \
  "v_mfma_f32_16x16x32_bf16 v[8:11], v[40:43], v[88:91], v[8:11]\n\t" \
  "ds_read_b128 v[56:59], v108 offset:24576\n\t" \
  "ds_read_b128 v[60:63], v108 offset:25600\n\t" \
  "v_mfma_f32_16x16x32_bf16 v[12:15], v[44:47], v[80:83], v[12:15]\n\t" \
  "ds_read_b128 v[64:67], v108 offset:26624\n\t" \
  "ds_read_b64_tr_b16 v[32:33], v112 offset:24576\n\t" \
  "v_mfma_f32_16x16x32_bf16 v[16:19], v[44:47], v[84:87], v[16:19]\n\t" \
  "ds_read_b64_tr_b16 v[34:35], v113 offset:24576\n\t" \
  "ds_read_b64_tr_b16 v[36:37], v114 offset:24576\n\t" \
  "v_mfma_f32_16x16x32_bf16 v[20:23], v[44:47], v[88:91], v[20:23]\n\t" \
  "ds_read_b64_tr_b16 v[38:39], v115 offset:24576\n\t" \
  "ds_read_b128 v[68:71], v109 offset:24576\n\t" \
  "v_mfma_f32_16x16x32_bf16 v[0:3], v[48:51], v[92:95], v[0:3]\n\t" \
  "ds_read_b128 v[72:75], v109 offset:25600\n\t" \
  "ds_read_b128 v[76:79], v109 offset:26624\n\t" \
  "v_mfma_f32_16x16x32_bf16 v[4:7], v[48:51], v[96:99], v[4:7]\n\t" \
  "v_mfma_f32_16x16x32_bf16 v[8:11], v[48:51], v[100:103], v[8:11]\n\t" \
  "v_mfma_f32_16x16x32_bf16 v[12:15], v[52:55], v[92:95], v[12:15]\n\t" \
  "v_mfma_f32_16x16x32_bf16 v[16:19], v[52:55], v[96:99], v[16:19]\n\t" \
  "v_mfma_f32_16x16x32_bf16 v[20:23], v[52:55], v[100:103], v[20:23]\n\t" \
  "s_waitcnt lgkmcnt(0)\n\t" \
  "s_barrier\n\t" \
  "v_mfma_f32_16x16x32_bf16 v[0:3], v[24:27], v[56:59], v[0:3]\n\t" \
  "ds_read_b64_tr_b16 v[40:41], v112 offset:32768\n\t" \
  "ds_read_b64_tr_b16 v[42:43], v113 offset:32768\n\t" \
  "v_mfma_f32_16x16x32_bf16 v[4:7], v[24:27], v[60:63], v[4:7]\n\t" \
  "ds_read_b64_tr_b16 v[44:45], v114 offset:32768\n\t" \
  "ds_read_b64_tr_b16 v[46:47], v115 offset:32768\n\t" \
  "v_mfma_f32_16x16x32_bf16 v[8:11], v[24:27], v[64:67], v[8:11]\n\t" \
  "ds_read_b128 v[80:83], v108 offset:30720\n\t" \
  "ds_read_b128 v[84:87], v108 offset:31744\n\t" \
  "v_mfma_f32_16x16x32_bf16 v[12:15], v[28:31], v[56:59], v[12:15]\n\t" \
  "ds_read_b128 v[88:91], v108 offset:32768\n\t" \
  "ds_read_b64_tr_b16 v[48:49], v112 offset:40960\n\t" \
  "v_mfma_f32_16x16x32_bf16 v[16:19], v[28:31], v[60:63], v[16:19]\n\t" \
  "ds_read_b64_tr_b16 v[50:51], v113 offset:40960\n\t" \
  "ds_read_b64_tr_b16 v[52:53], v114 offset:40960\n\t" \
  "v_mfma_f32_16x16x32_bf16 v[20:23], v[28:31], v[64:67], v[20:23]\n\t" \
  "ds_read_b64_tr_b16 v[54:55], v115 offset:40960\n\t" \
  "ds_read_b128 v[92:95], v109 offset:30720\n\t" \
  "v_mfma_f32_16x16x32_bf16 v[0:3], v[32:35], v[68:71], v[0:3]\n\t" \
  "ds_read_b128 v[96:99], v109 offset:31744\n\t" \
  "ds_read_b128 v[100:103], v109 offset:32768\n\t" \
  "v_mfma_f32_16x16x32_bf16 v[4:7], v[32:35], v[72:75], v[4:7]\n\t" \
  "v_mfma_f32_16x16x32_bf16 v[8:11], v[32:35], v[76:79], v[8:11]\n\t" \
  "v_mfma_f32_16x16x32_bf16 v[12:15], v[36:39], v[68:71], v[12:15]\n\t" \
  "v_mfma_f32_16x16x32_bf16 v[16:19], v[36:39], v[72:75], v[16:19]\n\t" \
  "v_mfma_f32_16x16x32_bf16 v[20:23], v[36:39], v[76:79], v[20:23]\n\t" \
  "s_waitcnt lgkmcnt(0)\n\t" \
  "s_barrier\n\t" \
  "v_mfma_f32_16x16x32_bf16 v[0:3], v[40:43], v[80:83], v[0:3]\n\t" \
  "ds_read_b64_tr_b16 v[24:25], v104 offset:0\n\t" \
  "ds_read_b64_tr_b16 v[26:27], v105 offset:0\n\t" \
  "v_mfma_f32_16x16x32_bf16 v[4:7], v[40:43], v[84:87], v[4:7]\n\t" \
  "ds_read_b64_tr_b16 v[28:29], v106 offset:0\n\t" \
  "ds_read_b64_tr_b16 v[30:31], v107 offset:0\n\t" \
  "v_mfma_f32_16x16x32_bf16 v[8:11], v[40:43], v[88:91], v[8:11]\n\t" \
  "ds_read_b128 v[56:59], v108 offset:36864\n\t" \
  "ds_read_b128 v[60:63], v108 offset:37888\n\t" \
  "v_mfma_f32_16x16x32_bf16 v[12:15], v[44:47], v[80:83], v[12:15]\n\t" \
  "ds_read_b128 v[64:67], v108 offset:38912\n\t" \
  "ds_read_b64_tr_b16 v[32:33], v104 offset:8192\n\t" \
  "v_mfma_f32_16x16x32_bf16 v[16:19], v[44:47], v[84:87], v[16:19]\n\t" \
  "ds_read_b64_tr_b16 v[34:35], v105 offset:8192\n\t" \
  "ds_read_b64_tr_b16 v[36:37], v106 offset:8192\n\t" \
  "v_mfma_f32_16x16x32_bf16 v[20:23], v[44:47], v[88:91], v[20:23]\n\t" \
  "ds_read_b64_tr_b16 v[38:39], v107 offset:8192\n\t" \
  "ds_read_b128 v[68:71], v109 offset:36864\n\t" \
  "v_mfma_f32_16x16x32_bf16 v[0:3], v[48:51], v[92:95], v[0:3]\n\t" \
  "ds_read_b128 v[72:75], v109 offset:37888\n\t" \
  "ds_read_b128 v[76:79], v109 offset:38912\n\t" \
  "v_mfma_f32_16x16x32_bf16 v[4:7], v[48:51], v[96:99], v[4:7]\n\t" \
  "v_mfma_f32_16x16x32_bf16 v[8:11], v[48:51], v[100:103], v[8:11]\n\t" \
  "v_mfma_f32_16x16x32_bf16 v[12:15], v[52:55], v[92:95], v[12:15]\n\t" \
  "v_mfma_f32_16x16x32_bf16 v[16:19], v[52:55], v[96:99], v[16:19]\n\t" \
  "v_mfma_f32_16x16x32_bf16 v[20:23], v[52:55], v[100:103], v[20:23]\n\t" \
  "s_waitcnt lgkmcnt(0)\n\t" \
  "s_barrier\n\t" \
  "v_mfma_f32_16x16x32_bf16 v[0:3], v[24:27], v[56:59], v[0:3]\n\t" \
  "ds_read_b64_tr_b16 v[40:41], v104 offset:16384\n\t" \
  "ds_read_b64_tr_b16 v[42:43], v105 offset:16384\n\t" \
  "v_mfma_f32_16x16x32_bf16 v[4:7], v[24:27], v[60:63], v[4:7]\n\t" \
  "ds_read_b64_tr_b16 v[44:45], v106 offset:16384\n\t" \
  "ds_read_b64_tr_b16 v[46:47], v107 offset:16384\n\t" \
  "v_mfma_f32_16x16x32_bf16 v[8:11], v[24:27], v[64:67], v[8:11]\n\t" \
  "ds_read_b128 v[80:83], v108 offset:43008\n\t" \
  "ds_read_b128 v[84:87], v108 offset:44032\n\t" \
  "v_mfma_f32_16x16x32_bf16 v[12:15], v[28:31], v[56:59], v[12:15]\n\t" \
  "ds_read_b128 v[88:91], v108 offset:45056\n\t" \
  "ds_read_b64_tr_b16 v[48:49], v104 offset:24576\n\t" \
  "v_mfma_f32_16x16x32_bf16 v[16:19], v[28:31], v[60:63], v[16:19]\n\t" \
  "ds_read_b64_tr_b16 v[50:51], v105 offset:24576\n\t" \
  "ds_read_b64_tr_b16 v[52:53], v106 offset:24576\n\t" \
  "v_mfma_f32_16x16x32_bf16 v[20:23], v[28:31], v[64:67], v[20:23]\n\t" \
  "ds_read_b64_tr_b16 v[54:55], v107 offset:24576\n\t" \
  "ds_read_b128 v[92:95], v109 offset:43008\n\t" \
  "v_mfma_f32_16x16x32_bf16 v[0:3], v[32:35], v[68:71], v[0:3]\n\t" \
  "ds_read_b128 v[96:99], v109 offset:44032\n\t" \
  "ds_read_b128 v[100:103], v109 offset:45056\n\t" \
  "v_mfma_f32_16x16x32_bf16 v[4:7], v[32:35], v[72:75], v[4:7]\n\t" \
  "v_mfma_f32_16x16x32_bf16 v[8:11], v[32:35], v[76:79], v[8:11]\n\t" \
  "v_mfma_f32_16x16x32_bf16 v[12:15], v[36:39], v[68:71], v[12:15]\n\t" \
  "v_mfma_f32_16x16x32_bf16 v[16:19], v[36:39], v[72:75], v[16:19]\n\t" \
  "v_mfma_f32_16x16x32_bf16 v[20:23], v[36:39], v[76:79], v[20:23]\n\t" \
  "s_waitcnt lgkmcnt(9)\n\t" \
  "v_mfma_f32_16x16x32_bf16 v[0:3], v[40:43], v[80:83], v[0:3]\n\t" \
  "s_waitcnt lgkmcnt(8)\n\t" \
  "v_mfma_f32_16x16x32_bf16 v[4:7], v[40:43], v[84:87], v[4:7]\n\t" \
  "s_waitcnt lgkmcnt(7)\n\t" \
  "v_mfma_f32_16x16x32_bf16 v[8:11], v[40:43], v[88:91], v[8:11]\n\t" \
  "v_mfma_f32_16x16x32_bf16 v[12:15], v[44:47], v[80:83], v[12:15]\n\t" \
  "v_mfma_f32_16x16x32_bf16 v[16:19], v[44:47], v[84:87], v[16:19]\n\t" \
  "v_mfma_f32_16x16x32_bf16 v[20:23], v[44:47], v[88:91], v[20:23]\n\t" \
  "s_waitcnt lgkmcnt(2)\n\t" \
  "v_mfma_f32_16x16x32_bf16 v[0:3], v[48:51], v[92:95], v[0:3]\n\t" \
  "s_waitcnt lgkmcnt(1)\n\t" \
  "v_mfma_f32_16x16x32_bf16 v[4:7], v[48:51], v[96:99], v[4:7]\n\t" \
  "s_waitcnt lgkmcnt(0)\n\t" \
  "v_mfma_f32_16x16x32_bf16 v[8:11], v[48:51], v[100:103], v[8:11]\n\t" \
  "v_mfma_f32_16x16x32_bf16 v[12:15], v[52:55], v[92:95], v[12:15]\n\t" \
  "v_mfma_f32_16x16x32_bf16 v[16:19], v[52:55], v[96:99], v[16:19]\n\t" \
  "v_mfma_f32_16x16x32_bf16 v[20:23], v[52:55], v[100:103], v[20:23]\n\t" \
  "s_nop 7\n\t" \
  "s_nop 1\n\t"
#define GEMM_CLOBBERS "v24", "v25", "v26", "v27", "v28", "v29", "v30", "v31", "v32", "v33", "v34", "v35", "v36", "v37", "v38", "v39", "v40", "v41", "v42", "v43", "v44", "v45", "v46", "v47", "v48", "v49", "v50", "v51", "v52", "v53", "v54", "v55", "v56", "v57", "v58", "v59", "v60", "v61", "v62", "v63", "v64", "v65", "v66", "v67", "v68", "v69", "v70", "v71", "v72", "v73", "v74", "v75", "v76", "v77", "v78", "v79", "v80", "v81", "v82", "v83", "v84", "v85", "v86", "v87", "v88", "v89", "v90", "v91", "v92", "v93", "v94", "v95", "v96", "v97", "v98", "v99", "v100", "v101", "v102", "v103"


constexpr unsigned LDS_X = 0;
constexpr unsigned LDS_W = 98304;
constexpr unsigned LDS_QS = 131072;
constexpr unsigned LDS_ST = 159744;
constexpr unsigned LDS_W2 = 0;
constexpr unsigned LDS_A2 = 114688;
constexpr unsigned LDS_TOTAL = 163840;
constexpr unsigned CODE_SPAN = 40960;
constexpr float CSCALE = 0.12751743082f;
constexpr size_t WS_OFRAG = 0;
constexpr size_t WS_PART = 4u << 20;
constexpr size_t WS_FLAGO = 12u << 20;
constexpr size_t WS_FLAGP = WS_FLAGO + 4096;
constexpr size_t WS_EPOCH = WS_FLAGP + 32768;

template <int XMODE>
DI void gemm_compute(f32x4 (&acc)[2][3], unsigned ldsb, int c, int lane) {
  const int g = lane >> 4, qq = (lane & 15) >> 2, p = lane & 3;
  u32x4 trb, xb;
#pragma unroll
  for (int j = 0; j < 2; ++j)
#pragma unroll
    for (int t = 0; t < 2; ++t)
      trb[2 * j + t] = (ldsb + (XMODE == 0 ? LDS_W : LDS_W2) + 2048 * g + 512 * c + 64 * (4 * t + qq) + 16 * ((p >> 1) ^ ((2 * g + t) & 3)) + 8 * (p & 1)) ^ (32 * j);
#pragma unroll
  for (int ks = 0; ks < 2; ++ks) {
    if constexpr (XMODE == 0) { const int row = lane & 15; xb[ks] = ldsb + LDS_X + row * 128 + 16 * ((4 * ks + g) ^ ((row >> 1) & 7)); }
    else xb[ks] = ldsb + LDS_A2 + ks * 3072 + lane * 16;
    xb[2 + ks] = xb[ks] + 49152;
  }
  f32x16 a0; f32x8 a1;
  if constexpr (XMODE == 0) asm volatile(GEMM_Q_ASM : "={v[0:15]}"(a0), "={v[16:23]}"(a1) : "{v[104:107]}"(trb), "{v[108:111]}"(xb) : "memory", GEMM_CLOBBERS);
  else {
    const u32x4 trh = trb + 65536u;
    asm volatile(GEMM_O_ASM : "={v[0:15]}"(a0), "={v[16:23]}"(a1) : "{v[104:107]}"(trb), "{v[108:111]}"(xb), "{v[112:115]}"(trh) : "memory", GEMM_CLOBBERS);
  }
#pragma unroll
  for (int i = 0; i < 4; ++i) {
    acc[0][0][i] = a0[i]; acc[0][1][i] = a0[4 + i]; acc[0][2][i] = a0[8 + i]; acc[1][0][i] = a0[12 + i];
    acc[1][1][i] = a1[i]; acc[1][2][i] = a1[4 + i];
  }
}
DI void issue_wtile(srd_t wsrd, unsigned wsoff0, unsigned wrow_stride, unsigned ldsdst, int li, int lane, int ktile) {
  const unsigned wv0 = ((lane >> 2) & 7) * wrow_stride + (lane >> 5) * 64 + 16 * ((lane & 3) ^ ((lane >> 4) & 1));
#pragma unroll
  for (int r = 0; r < 2; ++r) {
    const int R = li * 2 + r;
    const unsigned so = wsoff0 + (unsigned)(ktile * 64 + R * 8) * wrow_stride;
    const unsigned dst = ldsdst + R * 2048;
    const unsigned wv = wv0 ^ (32 * r);
    dma16(wsrd, wv, so, dst);
    dma16(wsrd, wv, so + 128, dst + 1024);
  }
}

DI void finalize_o(unsigned ldsb, char* ofrag_head, int w, int lane) {
  const int g = lane >> 4, r16 = lane & 15;
  f32x4 mw[3], lw[3];
  f32x4 pa[3][4];
#pragma unroll
  for (int qt = 0; qt < 3; ++qt) {
    mw[qt] = *(const LAS f32x4*)(size_t)(ldsb + LDS_ST + (qt * 16 + r16) * 16);
    lw[qt] = *(const LAS f32x4*)(size_t)(ldsb + LDS_ST + 768 + (qt * 16 + r16) * 16);
  }
#pragma unroll
  for (int qt = 0; qt < 3; ++qt)
#pragma unroll
    for (int s = 0; s < 4; ++s) pa[qt][s] = *(const LAS f32x4*)(size_t)(ldsb + ((w * 4 + s) * 3 + qt) * 1024 + lane * 16);
  srd_t os = mk_srd(ofrag_head, 4 * 3072);
#pragma unroll
  for (int qt = 0; qt < 3; ++qt) {
    const float M = fmaxf(fmaxf(mw[qt][0], mw[qt][1]), fmaxf(mw[qt][2], mw[qt][3]));
    f32x4 e;
#pragma unroll
    for (int s = 0; s < 4; ++s) e[s] = __builtin_amdgcn_exp2f(mw[qt][s] - M);
    const float il = __builtin_amdgcn_rcpf((lw[qt][0] * e[0] + lw[qt][1] * e[1]) + (lw[qt][2] * e[2] + lw[qt][3] * e[3]));
    const f32x4 a = ((pa[qt][0] * e[0] + pa[qt][1] * e[1]) + (pa[qt][2] * e[2] + pa[qt][3] * e[3])) * il;
    u32x2 w2 = {cvtpk(a[0], a[1]), cvtpk(a[2], a[3])};
    const unsigned fo = (unsigned)(((w >> 1) * 3 + qt) * 1024 + (r16 + 16 * (2 * (w & 1) + (g >> 1))) * 16 + 8 * (g & 1));
    __builtin_amdgcn_raw_buffer_store_b64(w2, os, fo, 0, 16);
    *(LAS u32x2*)(size_t)(ldsb + LDS_A2 + fo) = w2;
  }
}

__global__ __attribute__((amdgpu_num_vgpr(254))) __launch_bounds__(512, 2) void fused_kernel(const char* __restrict__ x, const char* __restrict__ kc, const char* __restrict__ vc,
                                                       const char* __restrict__ wq, const char* __restrict__ wo, char* __restrict__ out, char* __restrict__ ws) {
  const u64 pc0 = __builtin_amdgcn_s_getpc();
  extern __shared__ __attribute__((aligned(16))) char smem[];
  const unsigned ldsb = (unsigned)(size_t)smem;
  const int tid = threadIdx.x, lane = tid & 63;
  const int wid = (int)rfl(tid >> 6);
  const int bid = blockIdx.x, xcd = bid & 7, slot = bid >> 3;
  const int kvh = xcd >> 1, b = (xcd & 1) * 4 + (slot >> 3), hl = slot & 7, h = kvh * 8 + hl;
  const int g = lane >> 4, r16 = lane & 15;
  auto ktile_of = [&](int p) { return (p + 2 * (xcd & 1)) & 15; };
  asm volatile("" ::"s"(x), "s"(kc), "s"(vc), "s"(wq), "s"(wo), "s"(out), "s"(ws));
  const int li = wid - 4;
  srd_t wsr_q = mk_srd(wq, DM * HQ * DH * 2);
  srd_t xs_q = mk_srd(x + (size_t)b * SQ * DM * 2, SQ * DM * 2);
  auto issue_wq = [&](int p) { issue_wtile(wsr_q, h * 256, HQ * DH * 2, ldsb + LDS_W + (p & 3) * 16384, li, lane, ktile_of(p)); };
  auto issue_x = [&](int m) {
#pragma unroll
    for (int u = 0; u < 3; ++u) {
      const int P = li * 3 + u, p = 2 * m + (P >= 6), j = P >= 6 ? P - 6 : P;
      const unsigned voff = (lane >> 3) * 2048 + 16 * ((lane & 7) ^ ((4 * j + (lane >> 4)) & 7));
      dma16(xs_q, voff, (unsigned)(j * 8 * 2048 + ktile_of(p) * 128), ldsb + LDS_X + (m * 12 + P) * 1024);
    }
  };
  if (wid >= 4) {
    issue_wq(0); issue_x(0); issue_wq(1); issue_x(1); issue_wq(2); issue_wq(3);
#pragma unroll
    for (int m = 2; m < 8; ++m) issue_x(m);
  }
  u64* const flagO = (u64*)(ws + WS_FLAGO) + (b * 4 + kvh) * 8;
  u64* const epoch = (u64*)(ws + WS_EPOCH) + b * 16;

  if (wid >= 4) {
    srd_t ks_ = mk_srd(kc + (size_t)(b * HKV + kvh) * SK * DH * 2, SK * DH * 2);
    srd_t vs_ = mk_srd(vc + (size_t)(b * HKV + kvh) * SK * DH * 2, SK * DH * 2);
    const unsigned kv0 = (lane >> 4) * 256 + 16 * ((lane & 15) ^ (lane >> 4));
    const unsigned vv0 = ((lane >> 2) & 7) * 256 + (lane >> 5) * 64 + 16 * ((lane & 3) ^ ((lane >> 4) & 1));
    auto blk_of = [&](int t, int j) {
      const int pp = t >> 1;
      const int lg = (0x54732160u >> (4 * hl)) & 7;
      const int ps = lg > 2 ? lg - 2 : 0;
      const int G = pp == ps ? lg : (pp < ps ? pp : (pp <= lg ? pp - 1 : pp));
      return 8 * G + (t & 1) * 4 + j;
    };
    auto issue_kv = [&](int u, int j) {
      const int blk = blk_of(u >> 1, j);
      const unsigned dst = ldsb + (u & 3) * 32768 + j * 8192;
      if ((u & 1) == 0) {
#pragma unroll
        for (int q = 0; q < 8; ++q) dma16(ks_, kv0 ^ (64 * (q >> 1)), (unsigned)(blk * 32 + 4 * q) * 256, dst + q * 1024);
      } else {
#pragma unroll
        for (int q = 0; q < 8; ++q) dma16(vs_, vv0 ^ (32 * ((q >> 1) & 1)), (unsigned)(blk * 32 + 8 * (q >> 1)) * 256 + (q & 1) * 128, dst + q * 1024);
      }
    };
    {
      wait_vm<33>(); BARE_BARRIER();
      wait_vm<29>(); BARE_BARRIER(); issue_wq(4);
      wait_vm<26>(); BARE_BARRIER(); issue_wq(5);
      wait_vm<26>(); BARE_BARRIER(); issue_wq(6);
      wait_vm<8>();  BARE_BARRIER(); issue_wq(7);
      wait_vm<8>();  BARE_BARRIER(); issue_wq(8);
      wait_vm<8>();  BARE_BARRIER(); issue_wq(9);
      wait_vm<8>();  BARE_BARRIER(); issue_wq(10);
      wait_vm<8>();  BARE_BARRIER(); issue_wq(11);
      wait_vm<8>();  BARE_BARRIER(); issue_wq(12);
      wait_vm<8>();  BARE_BARRIER(); issue_wq(13);
      wait_vm<8>();  BARE_BARRIER(); issue_wq(14);
      wait_vm<8>();  BARE_BARRIER(); issue_wq(15); issue_kv(0, li);
      wait_vm<16>(); BARE_BARRIER(); issue_kv(1, li);
      wait_vm<20>(); BARE_BARRIER();
      wait_vm<16>(); BARE_BARRIER();
    }
    BARE_BARRIER();
    issue_kv(2, li);
    BARE_BARRIER();
    wait_vm<16>();
    BARE_BARRIER();
    wait_vm<8>();
    BARE_BARRIER();
    issue_kv(3, li);
    for (int u = 2; u < 30; ++u) {
      wait_vm<8>();
      BARE_BARRIER();
      issue_kv(u + 2, li);
    }
    {
      srd_t wsr = mk_srd(wo + (size_t)kvh * 1024 * DM * 2, 1024 * DM * 2);
#pragma unroll
      for (int i = 0; i < 2; ++i) touch(wsr, lane * 128, (unsigned)(slot * 65536 + (li * 2 + i) * 8192));
      wait_vm<10>(); BARE_BARRIER();
      wait_vm<2>(); BARE_BARRIER();
    }
    const int rot = 2 * hl;
    {
      srd_t wsr = mk_srd(wo, HQ * DH * DM * 2);
      const unsigned w0 = (unsigned)(kvh * 1024) * (DM * 2) + hl * 256;
      auto issue_w = [&](int p) { issue_wtile(wsr, w0, DM * 2, ldsb + LDS_W2 + (p % 7) * 16384, li, lane, (p + rot) & 15); };
      BARE_BARRIER();
      const u64 tag = *(const LAS u64*)(size_t)(ldsb + LDS_ST + 2048);
      finalize_o(ldsb, ws + WS_OFRAG + (size_t)(b * 128 + h * 4) * 3072, wid, lane);
      asm volatile("s_waitcnt vmcnt(0)" ::: "memory");
      BARRIER();
#pragma unroll
      for (int p = 0; p < 7; ++p) issue_w(p);
      srd_t os = mk_srd(ws + WS_OFRAG + (size_t)(b * 128 + kvh * 32) * 3072, 32 * 3072);
      auto issue_o = [&](int m) {
#pragma unroll
        for (int u = 0; u < 3; ++u) {
          const int P = li * 3 + u, p = 2 * m + (P >= 6), j = P >= 6 ? P - 6 : P;
          dma16<true>(os, lane * 16, (unsigned)(((p + rot) & 15) * 6 + j) * 1024, ldsb + LDS_A2 + ((m & 3) * 12 + P) * 1024);
        }
      };
      wait_vm<24>(); BARE_BARRIER();
      wait_vm<20>(); BARE_BARRIER(); issue_w(7);
      poll_flags(flagO, 8, tag, lane);
      issue_o(1); issue_o(2); issue_o(3);
      wait_vm<6>();  BARE_BARRIER(); issue_w(8); issue_o(4);
      BARE_BARRIER(); issue_w(9);
      wait_vm<14>(); BARE_BARRIER(); issue_w(10); issue_o(5);
      BARE_BARRIER(); issue_w(11);
      wait_vm<22>(); BARE_BARRIER(); issue_w(12); issue_o(6);
      BARE_BARRIER(); issue_w(13);
      wait_vm<26>(); BARE_BARRIER(); issue_w(14); issue_o(7);
      wait_vm<29>(); BARE_BARRIER(); issue_w(15);
      wait_vm<26>(); BARE_BARRIER();
      wait_vm<22>(); BARE_BARRIER();
      wait_vm<15>(); BARE_BARRIER();
      wait_vm<11>(); BARE_BARRIER();
      wait_vm<4>();  BARE_BARRIER();
      wait_vm<0>();  BARE_BARRIER();
    }
  } else {
    const int c = wid;
    u64 tag = ld_flag(epoch) + 1;
    f32x4 qacc[2][3];
    {
      if (c == 3) touch(mk_srd((const void*)(pc0 & ~(u64)0xff), CODE_SPAN), lane < 10 ? (unsigned)(slot * 10 + lane) * 128 : OOB, 0);
      if (c < 2) {
        const int idx = (slot * 2 + c) * 64 + lane, r = 256 + (idx >> 2);
        const unsigned off = (unsigned)(ktile_of((r >> 6) & 15) * 64 + (r & 63)) * (HQ * DH * 2) + kvh * 2048 + ((idx & 2) ? 5 : 1) * 256 + (idx & 1) * 128;
        touch(wsr_q, r < 1024 ? off : OOB, 0);
      }
      {
        if (c == 0) touch(mk_srd(kc + (size_t)(b * HKV + kvh) * SK * DH * 2, SK * DH * 2), lane < 8 ? (unsigned)lane * 65536u : OOB, hl * 8192);
        else if (c == 1) touch(mk_srd(vc + (size_t)(b * HKV + kvh) * SK * DH * 2, SK * DH * 2), lane < 8 ? (unsigned)lane * 65536u : OOB, hl * 8192);
        else if (c == 2) {
          touch(mk_srd(wo + (size_t)kvh * 1024 * DM * 2, 1024 * DM * 2), lane < 1 ? 0u : OOB, slot * 65536);
          touch(mk_srd(wq, DM * HQ * DH * 2), lane < 4 ? (unsigned)(slot * 4 + lane) * 65536u : OOB, kvh * 2048 + hl * 256);
        } else {
          touch(mk_srd(ws, 16u << 20),
                   lane < 2 ? (unsigned)(WS_OFRAG + (b * 128 + kvh * 32) * 3072 + lane * 65536) : lane < 4 ? (unsigned)(WS_PART + ((b * 8 + hl) * 3) * 32768 + (lane - 2) * 65536) : lane == 4 ? (unsigned)WS_FLAGO : OOB, 0);
          touch(mk_srd(out, B * SQ * DM * 2), lane < 2 ? (unsigned)(b * 48 * 2048 + lane * 65536 + hl * 256) : OOB, 0);
        }
      }
      gemm_compute<0>(qacc, ldsb, c, lane);
    }
    BARRIER();
#pragma unroll
    for (int j = 0; j < 2; ++j)
#pragma unroll
      for (int qt = 0; qt < 3; ++qt) {
        u32x2 w2 = {cvtpk(qacc[j][qt][0] * CSCALE, qacc[j][qt][1] * CSCALE), cvtpk(qacc[j][qt][2] * CSCALE, qacc[j][qt][3] * CSCALE)};
        const int q = 16 * qt + r16, ch = 2 * (2 * c + j) + (g >> 1);
        *(LAS u32x2*)(size_t)(ldsb + LDS_QS + q * 256 + 16 * (ch ^ r16) + 8 * (g & 1)) = w2;
      }
    BARRIER();
    u32x4 qa, ka, vb;
#pragma unroll
    for (int ks = 0; ks < 4; ++ks) {
      qa[ks] = ldsb + LDS_QS + r16 * 256 + 16 * ((4 * ks + g) ^ r16);
      ka[ks] = ldsb + c * 8192 + (8 * (r16 >> 2) + (r16 & 3)) * 256 + 16 * ((4 * ks + g) ^ r16);
    }
    {
      const int qq = r16 >> 2, p = lane & 3;
#pragma unroll
      for (int t = 0; t < 2; ++t)
#pragma unroll
        for (int e = 0; e < 2; ++e) vb[2 * t + e] = ldsb + 32768 + c * 8192 + 2048 * g + 64 * (4 * t + qq) + 16 * ((2 * e + (p >> 1)) ^ ((2 * g + t) & 3)) + 8 * (p & 1);
    }
    f32x32 o32[3];
    float m[3], l[3];
    {
      unsigned thr = 0xff800000u, flo = 0xff800000u, dk = 65536u, dv = 0u, cnt = 16u;
      asm volatile(ATTN_LOOP_ASM
                   : "={v[0:31]}"(o32[0]), "={v[32:63]}"(o32[1]), "={v[64:95]}"(o32[2]), "={v212}"(l[0]), "={v216}"(l[1]), "={v220}"(l[2]),
                     "={v200}"(m[0]), "={v204}"(m[1]), "={v208}"(m[2]), "+{v[176:179]}"(qa), "+{v[228:231]}"(ka), "+{v[232:235]}"(vb),
                     [thr] "+s"(thr), [flo] "+s"(flo), [dk] "+s"(dk), [dv] "+s"(dv), [cnt] "+s"(cnt)
                   :
                   : "memory", "vcc", "scc", ATTN_LOOP_CLOBBERS);
    }
    if (g == 0) {
#pragma unroll
      for (int qt = 0; qt < 3; ++qt) {
        *(LAS float*)(size_t)(ldsb + LDS_ST + (qt * 16 + r16) * 16 + c * 4) = -m[qt];
        *(LAS float*)(size_t)(ldsb + LDS_ST + 768 + (qt * 16 + r16) * 16 + c * 4) = l[qt];
      }
    }
    if (c == 0 && lane == 0) *(LAS u64*)(size_t)(ldsb + LDS_ST + 2048) = tag;
#pragma unroll
    for (int dt = 0; dt < 8; ++dt)
#pragma unroll
      for (int qt = 0; qt < 3; ++qt)
        *(LAS f32x4*)(size_t)(ldsb + ((dt * 4 + c) * 3 + qt) * 1024 + lane * 16) = f32x4{o32[qt][4 * dt], o32[qt][4 * dt + 1], o32[qt][4 * dt + 2], o32[qt][4 * dt + 3]};
    BARRIER();
    finalize_o(ldsb, ws + WS_OFRAG + (size_t)(b * 128 + h * 4) * 3072, wid, lane);
    asm volatile("s_waitcnt vmcnt(0)" ::: "memory");
    BARRIER();
    if (c == 0 && lane == 0) st_flag(flagO + hl, tag);
    f32x4 acc[2][3];
    gemm_compute<1>(acc, ldsb, c, lane);
    char* const pbase = ws + WS_PART + (size_t)((b * 8 + hl) * 3) * 4 * 4 * 2048;
    u64* const flagP = (u64*)(ws + WS_FLAGP) + (b * 8 + hl) * 48;
#pragma unroll
    for (int t = 0; t < 3; ++t) {
      if (t != kvh) {
        srd_t ps = mk_srd(pbase + (size_t)((t * 4 + kvh) * 4 + c) * 2048, 2048);
#pragma unroll
        for (int j = 0; j < 2; ++j) __builtin_amdgcn_raw_buffer_store_b128(__builtin_bit_cast(u32x4, acc[j][t]), ps, (unsigned)(j * 1024 + lane * 16), 0, 16);
      }
    }
    asm volatile("s_waitcnt vmcnt(0)" ::: "memory");
    if (lane < 3 && lane != kvh) st_flag(flagP + (lane * 4 + c) * 4 + kvh, tag);
    if (kvh < 3) {
      const u64* fp = flagP + (kvh * 4 + c) * 4;
      for (unsigned spins = 0; spins < (1u << 18); ++spins) {
        const u64 v = (lane < 4 && lane != kvh) ? ld_flag(fp + lane) : tag;
        if (__all(v == tag)) break;
        __builtin_amdgcn_s_sleep(1);
      }
      asm volatile("" ::: "memory");
      srd_t ps = mk_srd(pbase + (size_t)(kvh * 4) * 4 * 2048, 4 * 4 * 2048);
      u32x4 r[3][2];
#pragma unroll
      for (int s = 0; s < 3; ++s) {
        const int src = s < kvh ? s : s + 1;
#pragma unroll
        for (int j = 0; j < 2; ++j) r[s][j] = __builtin_amdgcn_raw_buffer_load_b128(ps, (unsigned)((src * 4 + c) * 2048 + j * 1024 + lane * 16), 0, 16);
      }
#pragma unroll
      for (int j = 0; j < 2; ++j) {
        f32x4 a = kvh == 0 ? acc[j][0] : (kvh == 1 ? acc[j][1] : acc[j][2]);
#pragma unroll
        for (int s = 0; s < 3; ++s) a += __builtin_bit_cast(f32x4, r[s][j]);
        u32x2 w2 = {cvtpk(a[0], a[1]), cvtpk(a[2], a[3])};
        *(u32x2*)(out + ((size_t)(b * 48 + 16 * kvh + r16) * 1024 + hl * 128 + (2 * c + j) * 16 + 4 * g) * 2) = w2;
      }
      if (hl == 0 && kvh == 0 && c == 0 && lane == 0) st_flag(epoch, tag);
    }
  }
}

static void launch(void* const* d_in, void* d_out, void* d_ws, hipStream_t stream) {
  (void)hipFuncSetAttribute((const void*)fused_kernel, hipFuncAttributeMaxDynamicSharedMemorySize, LDS_TOTAL);
  fused_kernel<<<256, 512, LDS_TOTAL, stream>>>((const char*)d_in[0], (const char*)d_in[1], (const char*)d_in[2], (const char*)d_in[3], (const char*)d_in[4], (char*)d_out, (char*)d_ws);
}
}

extern "C" void kernel_launch(void* const* d_in, const int* in_sizes, int n_in, void* d_out, int out_size, void* d_ws, size_t ws_size, hipStream_t stream) {
  k1::launch(d_in, d_out, d_ws, stream);
}
```
